```python
import torch

def _softmax(x, axis=-1):
    return torch.softmax(x, dim=axis)
def _gelu(x):
    return torch.nn.functional.gelu(x, approximate="tanh")
def _silu(x):
    return torch.nn.functional.silu(x)
def _sigmoid(x):
    return torch.sigmoid(x)
def _layernorm(x, axis=-1, eps=1e-5):
    m = x.mean(dim=axis, keepdim=True)
    v = x.var(dim=axis, keepdim=True, unbiased=False)
    return (x - m) / torch.sqrt(v + eps)
def _scatter_add(tgt, idx, src, axis=0):
    return tgt.clone().index_add_(axis, idx, src)
def _segment_sum(src, seg, n):
    out = torch.zeros((n,) + src.shape[1:], dtype=src.dtype)
    return out.index_add_(0, seg, src)
def _pad(x, pw):
    flat = tuple(v for pair in reversed(pw) for v in pair)
    return torch.nn.functional.pad(x, flat)

kernel_name = 'recurrent_scan__spectral_fnet__glu_gated_glu__prompt'

def setup_inputs():

    _w = (lambda a: (lambda t: t.to(torch.bfloat16) if t.is_floating_point() else t)(torch.as_tensor(a)))
    def _A_setup():
        B, S, D = 1, 2048, 576
        ns = torch.arange(S)[:, None] * torch.arange(S)[None, :]
        nd = torch.arange(D)[:, None] * torch.arange(D)[None, :]
        Ws_re = torch.cos(-2.0 * torch.pi * ns / S).to(float)
        Ws_im = torch.sin(-2.0 * torch.pi * ns / S).to(float)
        Wd_re = torch.cos(-2.0 * torch.pi * nd / D).to(float)
        Wd_im = torch.sin(-2.0 * torch.pi * nd / D).to(float)
        return {
            "x": _w((lambda *s: torch.randn(*s, dtype=torch.bfloat16) * 0.05)(B, S, D)),
            "dft_seq_re": _w(Ws_re),
            "dft_seq_im": _w(Ws_im),
            "dft_hid_re": _w(Wd_re),
            "dft_hid_im": _w(Wd_im),
        }
    _A_in = _A_setup()

    def _B_setup():
        B, S, D, F = 1, 2048, 576, 4608
        return {
            "x": _w((lambda *s: torch.randn(*s, dtype=torch.bfloat16) * 0.05)(B, S, D)),
            "w_gate": _w((lambda *s: torch.randn(*s, dtype=torch.bfloat16) * 0.05)(D, F)),
            "w_up": _w((lambda *s: torch.randn(*s, dtype=torch.bfloat16) * 0.05)(D, F)),
            "w_down": _w((lambda *s: torch.randn(*s, dtype=torch.bfloat16) * 0.05)(F, D)),
        }
    _B_in = _B_setup()

    return {
        "A_x": _A_in["x"],
        "A_dft_seq_re": _A_in["dft_seq_re"],
        "A_dft_seq_im": _A_in["dft_seq_im"],
        "A_dft_hid_re": _A_in["dft_hid_re"],
        "A_dft_hid_im": _A_in["dft_hid_im"],
        "B_x": _B_in["x"],
        "B_w_gate": _B_in["w_gate"],
        "B_w_up": _B_in["w_up"],
        "B_w_down": _B_in["w_down"],
    }

def reference(A_x, A_dft_seq_re, A_dft_seq_im, A_dft_hid_re, A_dft_hid_im, B_x, B_w_gate, B_w_up, B_w_down):
    def _A_ref(x, dft_seq_re, dft_seq_im, dft_hid_re, dft_hid_im):
        a = torch.einsum("bsd,ed->bse", x, dft_hid_re)
        b = torch.einsum("bsd,ed->bse", x, dft_hid_im)
        y = torch.einsum("ts,bse->bte", dft_seq_re, a) - torch.einsum("ts,bse->bte", dft_seq_im, b)
        return y

    def _B_ref(x, w_gate, w_up, w_down):
        g = torch.einsum("bsd,df->bsf", x, w_gate)
        u = torch.einsum("bsd,df->bsf", x, w_up)
        h = _sigmoid(g) * u
        return torch.einsum("bsf,fd->bsd", h, w_down)

    x = A_x
    xn = _layernorm(x, axis=-1)
    s = _A_ref(xn, A_dft_seq_re, A_dft_seq_im, A_dft_hid_re, A_dft_hid_im)
    g = _silu(_B_ref(xn, B_w_gate, B_w_up, B_w_down))
    return x + s * g
```

```cpp
#include <hip/hip_runtime.h>
#include <hip/hip_bf16.h>

typedef unsigned short bf16_t;
typedef short bf16x8 __attribute__((ext_vector_type(8)));
typedef short s16x4 __attribute__((ext_vector_type(4)));
typedef short v4i16_t __attribute__((ext_vector_type(4)));
typedef float f32x4 __attribute__((ext_vector_type(4)));
typedef unsigned u32x2 __attribute__((ext_vector_type(2)));
typedef unsigned u32x4 __attribute__((ext_vector_type(4)));

#define LAS __attribute__((address_space(3)))

constexpr int S_ = 2048, D_ = 576, F_ = 4608;

__device__ __forceinline__ float bf2f(unsigned short u) { return __uint_as_float(((unsigned)u) << 16); }
__device__ __forceinline__ unsigned short f2bf(float f) { return __builtin_bit_cast(unsigned short, (__bf16)f); }
__device__ __forceinline__ float rbf(float f) { return bf2f(f2bf(f)); }
__device__ __forceinline__ unsigned pack2(float lo, float hi) { return (unsigned)f2bf(lo) | ((unsigned)f2bf(hi) << 16); }

__device__ __forceinline__ s16x4 tr_read(const char* p) {
    return __builtin_bit_cast(s16x4, __builtin_amdgcn_ds_read_tr16_b64_v4i16((LAS v4i16_t*)p));
}
__device__ __forceinline__ bf16x8 tr_frag(const char* p0, const char* p1) {
    s16x4 a = tr_read(p0), b = tr_read(p1);
    bf16x8 r;
    r[0] = a[0]; r[1] = a[1]; r[2] = a[2]; r[3] = a[3];
    r[4] = b[0]; r[5] = b[1]; r[6] = b[2]; r[7] = b[3];
    return r;
}
__device__ __forceinline__ void glds16(const void* g, char* l) {
    __builtin_amdgcn_global_load_lds((const unsigned*)g, (LAS unsigned*)l, 16, 0, 0);
}
#define WAIT_V0() asm volatile("s_waitcnt vmcnt(0)" ::: "memory")

__global__ __launch_bounds__(256) void ln_kernel(const bf16_t* __restrict__ x, bf16_t* __restrict__ xn) {
    const int row = blockIdx.x * 4 + (threadIdx.x >> 6), lane = threadIdx.x & 63;
    const bf16_t* xr = x + (size_t)row * D_;
    bf16x8 v0 = *(const bf16x8*)(xr + lane * 8);
    bf16x8 v1 = {0, 0, 0, 0, 0, 0, 0, 0};
    const bool has2 = lane < 8;
    if (has2) v1 = *(const bf16x8*)(xr + 512 + lane * 8);
    float f[16];
    float s = 0.f;
#pragma unroll
    for (int j = 0; j < 8; ++j) { f[j] = bf2f((unsigned short)v0[j]); f[8 + j] = bf2f((unsigned short)v1[j]); s += f[j] + f[8 + j]; }
#pragma unroll
    for (int o = 32; o > 0; o >>= 1) s += __shfl_xor(s, o);
    const float mean = s * (1.0f / D_);
    float q = 0.f;
#pragma unroll
    for (int j = 0; j < 8; ++j) { float d0 = f[j] - mean; q += d0 * d0; if (has2) { float d1 = f[8 + j] - mean; q += d1 * d1; } }
#pragma unroll
    for (int o = 32; o > 0; o >>= 1) q += __shfl_xor(q, o);
    const float var = q * (1.0f / D_);
    const float mb = rbf(mean), vb = rbf(var);
    const float den = rbf(sqrtf(rbf(vb + 1e-5f)));
    bf16x8 o0, o1;
#pragma unroll
    for (int j = 0; j < 8; ++j) {
        o0[j] = (short)f2bf(rbf(f[j] - mb) / den);
        o1[j] = (short)f2bf(rbf(f[8 + j] - mb) / den);
    }
    bf16_t* orow = xn + (size_t)row * D_;
    *(bf16x8*)(orow + lane * 8) = o0;
    if (has2) *(bf16x8*)(orow + 512 + lane * 8) = o1;
}

__global__ __launch_bounds__(256, 2) void stage1_kernel(const bf16_t* __restrict__ xn, const bf16_t* __restrict__ wg,
                                                        const bf16_t* __restrict__ wu, const bf16_t* __restrict__ wre,
                                                        const bf16_t* __restrict__ wim, bf16_t* __restrict__ hbuf,
                                                        bf16_t* __restrict__ abT) {
    __shared__ __attribute__((aligned(1024))) char shm[2 * 32768];
    const int tid = threadIdx.x, wid = __builtin_amdgcn_readfirstlane(tid >> 6), lane = tid & 63;
    const int xcd = blockIdx.x & 7, idx = blockIdx.x >> 3;
    const int wr = wid >> 1, wc = wid & 1;
    const int fr = lane & 15, fq = lane >> 4;
    constexpr int NT = D_ / 64;

    int m0;
    const bool is_dft = idx < 18;
    int et = 0, ft = 0;
    if (is_dft) { int t = xcd * 18 + idx; et = t % 9; m0 = (t / 9) * 128; }
    else { int j = idx - 18; ft = xcd * 9 + (j >> 4); m0 = (j & 15) * 128; }
    const int srow = lane >> 3, schunk = (lane & 7) ^ srow;
    const bf16_t* a_src = xn + (size_t)(m0 + wid * 32 + srow) * D_ + schunk * 8;
    const int a_rd = (wr * 64 + fr) * 128;
    const int xr7 = lane & 7;

    if (!is_dft) {
        const int f0 = ft * 64;
        const bf16_t* bsrc[2][2];
#pragma unroll
        for (int ii = 0; ii < 2; ++ii) {
            const int i = 2 * wid + ii, k = 8 * i + srow;
            const int pos = lane & 7, c32p = pos >> 1;
            const int hh = ((k >> 1) & 1) | (((k >> 3) & 1) << 1);
            const int c16 = 2 * (c32p ^ hh) + (pos & 1);
            bsrc[0][ii] = wg + (size_t)k * F_ + f0 + c16 * 8;
            bsrc[1][ii] = wu + (size_t)k * F_ + f0 + c16 * 8;
        }
        auto stage = [&](int buf, int kt) {
            char* base = shm + buf * 32768;
#pragma unroll
            for (int i = 0; i < 4; ++i) glds16(a_src + (size_t)i * 8 * D_ + kt * 64, base + (wid * 4 + i) * 1024);
#pragma unroll
            for (int ii = 0; ii < 2; ++ii) {
                glds16(bsrc[0][ii] + (size_t)kt * 64 * F_, base + 16384 + (2 * wid + ii) * 1024);
                glds16(bsrc[1][ii] + (size_t)kt * 64 * F_, base + 24576 + (2 * wid + ii) * 1024);
            }
        };
        const int g = lane >> 4, q = (lane & 15) >> 2, p = lane & 3;
        const int hh = ((q >> 1) & 1) | ((g & 1) << 1);
        int b_rd[2];
#pragma unroll
        for (int n = 0; n < 2; ++n) b_rd[n] = (8 * g + q) * 128 + (((2 * wc + n) ^ hh) * 32) + p * 8;
        f32x4 accg[2][4] = {}, accu[2][4] = {};
        stage(0, 0); WAIT_V0(); __syncthreads();
        for (int t = 0; t < NT; ++t) {
            const int cur = t & 1;
            if (t + 1 < NT) stage(cur ^ 1, t + 1);
            const char* base = shm + cur * 32768;
#pragma unroll
            for (int ks = 0; ks < 2; ++ks) {
                bf16x8 af[4], bg[2], bu[2];
#pragma unroll
                for (int mt = 0; mt < 4; ++mt) af[mt] = *(const bf16x8*)(base + a_rd + mt * 2048 + (((ks * 4 + fq) ^ xr7) * 16));
#pragma unroll
                for (int n = 0; n < 2; ++n) {
                    const char* pb = base + 16384 + b_rd[n] + ks * 32 * 128;
                    bg[n] = tr_frag(pb, pb + 4 * 128);
                    bu[n] = tr_frag(pb + 8192, pb + 8192 + 4 * 128);
                }
#pragma unroll
                for (int n = 0; n < 2; ++n)
#pragma unroll
                    for (int mt = 0; mt < 4; ++mt) {
                        accg[n][mt] = __builtin_amdgcn_mfma_f32_16x16x32_bf16(bg[n], af[mt], accg[n][mt], 0, 0, 0);
                        accu[n][mt] = __builtin_amdgcn_mfma_f32_16x16x32_bf16(bu[n], af[mt], accu[n][mt], 0, 0, 0);
                    }
            }
            WAIT_V0(); __syncthreads();
        }
#pragma unroll
        for (int mt = 0; mt < 4; ++mt)
#pragma unroll
            for (int n = 0; n < 2; ++n) {
                float hv[4];
#pragma unroll
                for (int r = 0; r < 4; ++r) {
                    const float gv = rbf(accg[n][mt][r]), uv = rbf(accu[n][mt][r]);
                    const float sg = rbf(__builtin_amdgcn_rcpf(1.0f + __expf(-gv)));
                    hv[r] = sg * uv;
                }
                u32x2 o; o[0] = pack2(hv[0], hv[1]); o[1] = pack2(hv[2], hv[3]);
                const int tok = m0 + wr * 64 + mt * 16 + fr, f = f0 + wc * 32 + n * 16 + fq * 4;
                *(u32x2*)(hbuf + (size_t)tok * F_ + f) = o;
            }
    } else {
        const int e0 = et * 128;
        const bf16_t* bsrc[4];
#pragma unroll
        for (int i = 0; i < 4; ++i) {
            const int e = e0 + (wid * 4 + i) * 8 + srow;
            bsrc[i] = (e < D_ ? wre + (size_t)e * D_ : wim + (size_t)(e - D_) * D_) + schunk * 8;
        }
        auto stage = [&](int buf, int kt) {
            char* base = shm + buf * 32768;
#pragma unroll
            for (int i = 0; i < 4; ++i) glds16(a_src + (size_t)i * 8 * D_ + kt * 64, base + (wid * 4 + i) * 1024);
#pragma unroll
            for (int i = 0; i < 4; ++i) glds16(bsrc[i] + kt * 64, base + 16384 + (wid * 4 + i) * 1024);
        };
        const int b_rd = (wc * 64 + fr) * 128;
        f32x4 acc[4][4] = {};
        stage(0, 0); WAIT_V0(); __syncthreads();
        for (int t = 0; t < NT; ++t) {
            const int cur = t & 1;
            if (t + 1 < NT) stage(cur ^ 1, t + 1);
            const char* base = shm + cur * 32768;
#pragma unroll
            for (int ks = 0; ks < 2; ++ks) {
                bf16x8 af[4], bf[4];
                const int co = ((ks * 4 + fq) ^ xr7) * 16;
#pragma unroll
                for (int mt = 0; mt < 4; ++mt) af[mt] = *(const bf16x8*)(base + a_rd + mt * 2048 + co);
#pragma unroll
                for (int n = 0; n < 4; ++n) bf[n] = *(const bf16x8*)(base + 16384 + b_rd + n * 2048 + co);
#pragma unroll
                for (int mt = 0; mt < 4; ++mt)
#pragma unroll
                    for (int n = 0; n < 4; ++n)
                        acc[mt][n] = __builtin_amdgcn_mfma_f32_16x16x32_bf16(af[mt], bf[n], acc[mt][n], 0, 0, 0);
            }
            WAIT_V0(); __syncthreads();
        }
#pragma unroll
        for (int mt = 0; mt < 4; ++mt)
#pragma unroll
            for (int n = 0; n < 4; ++n) {
                u32x2 o; o[0] = pack2(acc[mt][n][0], acc[mt][n][1]); o[1] = pack2(acc[mt][n][2], acc[mt][n][3]);
                const int e = e0 + wc * 64 + n * 16 + fr, tok = m0 + wr * 64 + mt * 16 + fq * 4;
                *(u32x2*)(abT + (size_t)e * S_ + tok) = o;
            }
    }
}

__global__ __launch_bounds__(256, 2) void stage2_kernel(const bf16_t* __restrict__ cre, const bf16_t* __restrict__ cim,
                                                        const bf16_t* __restrict__ abT, const bf16_t* __restrict__ hbuf,
                                                        const bf16_t* __restrict__ wd, bf16_t* __restrict__ ybuf,
                                                        float* __restrict__ dbuf) {
    constexpr int STG = 16384 + 18432;
    __shared__ __attribute__((aligned(1024))) char shm[2 * STG];
    const int tid = threadIdx.x, wid = __builtin_amdgcn_readfirstlane(tid >> 6), lane = tid & 63;
    const int xcd = blockIdx.x & 7, idx = blockIdx.x >> 3;
    const int ty = xcd & 3, m0 = ((xcd >> 2) * 8 + (idx >> 2)) * 128, n0 = (idx & 3) * 144;
    const int fr = lane & 15, fq = lane >> 4, xr7 = lane & 7;
    const int srow = lane >> 3, schunk = (lane & 7) ^ srow;
    const bool nn = ty >= 2;
    const int lda = nn ? F_ : S_;
    const int nkt = nn ? 36 : 32;
    const bf16_t* abase = ty == 0 ? cre : (ty == 1 ? cim : (ty == 2 ? hbuf : hbuf + 2304));
    const bf16_t* a_src = abase + (size_t)(m0 + wid * 32 + srow) * lda + schunk * 8;
    const int nb = wid < 2 ? 5 : 4;
    const bf16_t* bsrc[5];
    if (!nn) {
#pragma unroll
        for (int j = 0; j < 5; ++j) {
            int i = wid + 4 * j; if (i > 17) i = 17;
            bsrc[j] = abT + (size_t)(ty * D_ + n0 + i * 8 + srow) * S_ + schunk * 8;
        }
    } else {
#pragma unroll
        for (int j = 0; j < 5; ++j) {
            int i = wid + 4 * j; if (i > 17) i = 17;
            const int P = i * 64 + lane, k = P / 18, c16p = P % 18, c32p = c16p >> 1;
            const int c32 = (c32p + 9 - 4 * ((k >> 3) & 1)) % 9;
            bsrc[j] = wd + (size_t)((ty - 2) * 2304 + k) * D_ + n0 + (2 * c32 + (c16p & 1)) * 8;
        }
    }
    const size_t bstep = nn ? (size_t)64 * D_ : (size_t)64;
    auto stage = [&](int buf, int kt) {
        char* base = shm + buf * STG;
#pragma unroll
        for (int i = 0; i < 4; ++i) glds16(a_src + (size_t)i * 8 * lda + kt * 64, base + (wid * 4 + i) * 1024);
#pragma unroll
        for (int j = 0; j < 5; ++j)
            if (j < nb) glds16(bsrc[j] + (size_t)kt * bstep, base + 16384 + (wid + 4 * j) * 1024);
    };
    const int a_rd = (wid * 32 + fr) * 128;
    const int g = lane >> 4, q = (lane & 15) >> 2, p = lane & 3;
    f32x4 acc[9][2] = {};
    stage(0, 0); WAIT_V0(); __syncthreads();
    if (!nn) {
        for (int t = 0; t < nkt; ++t) {
            const int cur = t & 1;
            if (t + 1 < nkt) stage(cur ^ 1, t + 1);
            const char* base = shm + cur * STG;
#pragma unroll
            for (int ks = 0; ks < 2; ++ks) {
                const int co = ((ks * 4 + fq) ^ xr7) * 16;
                bf16x8 af[2], bf[9];
#pragma unroll
                for (int mt = 0; mt < 2; ++mt) af[mt] = *(const bf16x8*)(base + a_rd + mt * 2048 + co);
#pragma unroll
                for (int n = 0; n < 9; ++n) bf[n] = *(const bf16x8*)(base + 16384 + (n * 16 + fr) * 128 + co);
#pragma unroll
                for (int n = 0; n < 9; ++n)
#pragma unroll
                    for (int mt = 0; mt < 2; ++mt)
                        acc[n][mt] = __builtin_amdgcn_mfma_f32_16x16x32_bf16(bf[n], af[mt], acc[n][mt], 0, 0, 0);
            }
            WAIT_V0(); __syncthreads();
        }
    } else {
        const int rb = (8 * g + q) * 288 + p * 8;
        const int sh = 4 * (g & 1);
        for (int t = 0; t < nkt; ++t) {
            const int cur = t & 1;
            if (t + 1 < nkt) stage(cur ^ 1, t + 1);
            const char* base = shm + cur * STG;
#pragma unroll
            for (int ks = 0; ks < 2; ++ks) {
                const int co = ((ks * 4 + fq) ^ xr7) * 16;
                bf16x8 af[2], bf[9];
#pragma unroll
                for (int mt = 0; mt < 2; ++mt) af[mt] = *(const bf16x8*)(base + a_rd + mt * 2048 + co);
#pragma unroll
                for (int n = 0; n < 9; ++n) {
                    int c = n + sh; if (c >= 9) c -= 9;
                    const char* pb = base + 16384 + rb + ks * 32 * 288 + c * 32;
                    bf[n] = tr_frag(pb, pb + 4 * 288);
                }
#pragma unroll
                for (int n = 0; n < 9; ++n)
#pragma unroll
                    for (int mt = 0; mt < 2; ++mt)
                        acc[n][mt] = __builtin_amdgcn_mfma_f32_16x16x32_bf16(bf[n], af[mt], acc[n][mt], 0, 0, 0);
            }
            WAIT_V0(); __syncthreads();
        }
    }
#pragma unroll
    for (int mt = 0; mt < 2; ++mt)
#pragma unroll
        for (int n = 0; n < 9; ++n) {
            const int tok = m0 + wid * 32 + mt * 16 + fr, d = n0 + n * 16 + fq * 4;
            if (!nn) {
                u32x2 o; o[0] = pack2(acc[n][mt][0], acc[n][mt][1]); o[1] = pack2(acc[n][mt][2], acc[n][mt][3]);
                *(u32x2*)(ybuf + (size_t)ty * S_ * D_ + (size_t)tok * D_ + d) = o;
            } else {
                *(f32x4*)(dbuf + (size_t)(ty - 2) * S_ * D_ + (size_t)tok * D_ + d) = acc[n][mt];
            }
        }
}

__global__ __launch_bounds__(256) void combine_kernel(const bf16_t* __restrict__ x, const bf16_t* __restrict__ ybuf,
                                                      const float* __restrict__ dbuf, bf16_t* __restrict__ out) {
    const int i = (blockIdx.x * 256 + threadIdx.x) * 8;
    if (i >= S_ * D_) return;
    const bf16x8 xv = *(const bf16x8*)(x + i);
    const bf16x8 y1 = *(const bf16x8*)(ybuf + i), y2 = *(const bf16x8*)(ybuf + S_ * D_ + i);
    f32x4 dl[2], dh[2];
    dl[0] = *(const f32x4*)(dbuf + i); dl[1] = *(const f32x4*)(dbuf + i + 4);
    dh[0] = *(const f32x4*)(dbuf + S_ * D_ + i); dh[1] = *(const f32x4*)(dbuf + S_ * D_ + i + 4);
    bf16x8 o;
#pragma unroll
    for (int j = 0; j < 8; ++j) {
        const float s = rbf(bf2f((unsigned short)y1[j]) - bf2f((unsigned short)y2[j]));
        const float d = rbf(dl[j >> 2][j & 3] + dh[j >> 2][j & 3]);
        const float sl = rbf(d * __builtin_amdgcn_rcpf(1.0f + __expf(-d)));
        o[j] = (short)f2bf(bf2f((unsigned short)xv[j]) + rbf(s * sl));
    }
    *(bf16x8*)(out + i) = o;
}

extern "C" void kernel_launch(void* const* d_in, const int* in_sizes, int n_in,
                              void* d_out, int out_size, void* d_ws, size_t ws_size,
                              hipStream_t stream) {
    const bf16_t* x = (const bf16_t*)d_in[0];
    const bf16_t* cre = (const bf16_t*)d_in[1];
    const bf16_t* cim = (const bf16_t*)d_in[2];
    const bf16_t* wre = (const bf16_t*)d_in[3];
    const bf16_t* wim = (const bf16_t*)d_in[4];
    const bf16_t* wg = (const bf16_t*)d_in[6];
    const bf16_t* wu = (const bf16_t*)d_in[7];
    const bf16_t* wd = (const bf16_t*)d_in[8];
    bf16_t* out = (bf16_t*)d_out;
    char* ws = (char*)d_ws;
    bf16_t* xn = (bf16_t*)ws;                       ws += (size_t)S_ * D_ * 2;
    bf16_t* abT = (bf16_t*)ws;                      ws += (size_t)2 * D_ * S_ * 2;
    bf16_t* hbuf = (bf16_t*)ws;                     ws += (size_t)S_ * F_ * 2;
    bf16_t* ybuf = (bf16_t*)ws;                     ws += (size_t)2 * S_ * D_ * 2;
    float* dbuf = (float*)ws;                       ws += (size_t)2 * S_ * D_ * 4;

    ln_kernel<<<S_ / 4, 256, 0, stream>>>(x, xn);
    stage1_kernel<<<1296, 256, 0, stream>>>(xn, wg, wu, wre, wim, hbuf, abT);
    stage2_kernel<<<256, 256, 0, stream>>>(cre, cim, abT, hbuf, wd, ybuf, dbuf);
    combine_kernel<<<(S_ * D_ / 8 + 255) / 256, 256, 0, stream>>>(x, ybuf, dbuf, out);
}
```

```cpp
#include <hip/hip_runtime.h>
#include <hip/hip_bf16.h>

typedef unsigned short bf16_t;
typedef short bf16x8 __attribute__((ext_vector_type(8)));
typedef short s16x4 __attribute__((ext_vector_type(4)));
typedef short v4i16_t __attribute__((ext_vector_type(4)));
typedef float f32x4 __attribute__((ext_vector_type(4)));
typedef unsigned u32x2 __attribute__((ext_vector_type(2)));
typedef unsigned u32x4 __attribute__((ext_vector_type(4)));

#define LAS __attribute__((address_space(3)))

constexpr int S_ = 2048, D_ = 576, F_ = 4608;
#define PHASE_MODE 1

__device__ __forceinline__ float bf2f(unsigned short u) { return __uint_as_float(((unsigned)u) << 16); }
__device__ __forceinline__ unsigned short f2bf(float f) { return __builtin_bit_cast(unsigned short, (__bf16)f); }
__device__ __forceinline__ float rbf(float f) { return bf2f(f2bf(f)); }
__device__ __forceinline__ unsigned pack2(float lo, float hi) { return (unsigned)f2bf(lo) | ((unsigned)f2bf(hi) << 16); }

__device__ __forceinline__ s16x4 tr_read(const char* p) {
    return __builtin_bit_cast(s16x4, __builtin_amdgcn_ds_read_tr16_b64_v4i16((LAS v4i16_t*)p));
}
__device__ __forceinline__ bf16x8 tr_frag(const char* p0, const char* p1) {
    s16x4 a = tr_read(p0), b = tr_read(p1);
    bf16x8 r;
    r[0] = a[0]; r[1] = a[1]; r[2] = a[2]; r[3] = a[3];
    r[4] = b[0]; r[5] = b[1]; r[6] = b[2]; r[7] = b[3];
    return r;
}
__device__ __forceinline__ void glds16(const void* g, char* l) {
    __builtin_amdgcn_global_load_lds((const unsigned*)g, (LAS unsigned*)l, 16, 0, 0);
}
#define WAIT_V0() asm volatile("s_waitcnt vmcnt(0)" ::: "memory")

constexpr int PREP_LN = S_ / 4, PREP_TR = (F_ / 64) * (D_ / 64), PREP_PFW = D_ * F_ * 2 / 32768, PREP_PFH = (D_ * D_ * 2 + 32767) / 32768;
__global__ __launch_bounds__(256) void ln_kernel(const bf16_t* __restrict__ x, bf16_t* __restrict__ xn,
                                                 const bf16_t* __restrict__ p0, const bf16_t* __restrict__ p1,
                                                 const bf16_t* __restrict__ p2, const bf16_t* __restrict__ p3,
                                                 const bf16_t* __restrict__ wd, bf16_t* __restrict__ wdT, unsigned* __restrict__ cnt) {
    __shared__ unsigned short tile[64][66];
    if (blockIdx.x == 0) { for (int i = threadIdx.x; i < 64 * 32; i += 256) cnt[i] = 0u; }
    if (blockIdx.x >= PREP_LN + PREP_TR) {
        int bb = blockIdx.x - PREP_LN - PREP_TR;
        const char* base; size_t lim;
        if (bb < PREP_PFW) { base = (const char*)p0; lim = (size_t)D_ * F_ * 2; }
        else if ((bb -= PREP_PFW) < PREP_PFW) { base = (const char*)p1; lim = (size_t)D_ * F_ * 2; }
        else if ((bb -= PREP_PFW) < PREP_PFH) { base = (const char*)p2; lim = (size_t)D_ * D_ * 2; }
        else { bb -= PREP_PFH; base = (const char*)p3; lim = (size_t)D_ * D_ * 2; }
        u32x4 v[8];
#pragma unroll
        for (int i = 0; i < 8; ++i) {
            size_t o = (size_t)bb * 32768 + i * 4096 + threadIdx.x * 16;
            if (o >= lim) o = 0;
            v[i] = *(const u32x4*)(base + o);
        }
#pragma unroll
        for (int i = 0; i < 8; ++i) asm volatile("" ::"v"(v[i]));
        return;
    }
    if (blockIdx.x >= PREP_LN) {
        const int b = blockIdx.x - PREP_LN, f0 = (b / 9) * 64, d0 = (b % 9) * 64;
        const int r = threadIdx.x >> 3, c = (threadIdx.x & 7) * 8;
#pragma unroll
        for (int h = 0; h < 2; ++h) {
            const bf16x8 v = *(const bf16x8*)(wd + (size_t)(f0 + r + 32 * h) * D_ + d0 + c);
#pragma unroll
            for (int j = 0; j < 8; ++j) tile[r + 32 * h][c + j] = (unsigned short)v[j];
        }
        __syncthreads();
#pragma unroll
        for (int h = 0; h < 2; ++h) {
            bf16x8 v;
#pragma unroll
            for (int j = 0; j < 8; ++j) v[j] = (short)tile[c + j][r + 32 * h];
            *(bf16x8*)(wdT + (size_t)(d0 + r + 32 * h) * F_ + f0 + c) = v;
        }
        return;
    }
    const int row = blockIdx.x * 4 + (threadIdx.x >> 6), lane = threadIdx.x & 63;
    const bf16_t* xr = x + (size_t)row * D_;
    bf16x8 v0 = *(const bf16x8*)(xr + lane * 8);
    bf16x8 v1 = {0, 0, 0, 0, 0, 0, 0, 0};
    const bool has2 = lane < 8;
    if (has2) v1 = *(const bf16x8*)(xr + 512 + lane * 8);
    float f[16];
    float s = 0.f;
#pragma unroll
    for (int j = 0; j < 8; ++j) { f[j] = bf2f((unsigned short)v0[j]); f[8 + j] = bf2f((unsigned short)v1[j]); s += f[j] + f[8 + j]; }
#pragma unroll
    for (int o = 32; o > 0; o >>= 1) s += __shfl_xor(s, o);
    const float mean = s * (1.0f / D_);
    float q = 0.f;
#pragma unroll
    for (int j = 0; j < 8; ++j) { float d0 = f[j] - mean; q += d0 * d0; if (has2) { float d1 = f[8 + j] - mean; q += d1 * d1; } }
#pragma unroll
    for (int o = 32; o > 0; o >>= 1) q += __shfl_xor(q, o);
    const float var = q * (1.0f / D_);
    const float mb = rbf(mean), vb = rbf(var);
    const float den = rbf(sqrtf(rbf(vb + 1e-5f)));
    bf16x8 o0, o1;
#pragma unroll
    for (int j = 0; j < 8; ++j) {
        o0[j] = (short)f2bf(rbf(f[j] - mb) / den);
        o1[j] = (short)f2bf(rbf(f[8 + j] - mb) / den);
    }
    bf16_t* orow = xn + (size_t)row * D_;
    *(bf16x8*)(orow + lane * 8) = o0;
    if (has2) *(bf16x8*)(orow + 512 + lane * 8) = o1;
}

#define LGKM0() asm volatile("s_waitcnt lgkmcnt(0)" ::: "memory")
#define SB0() __builtin_amdgcn_sched_barrier(0)
#define DSR128(dst, addr, off) asm volatile("ds_read_b128 %0, %1 offset:%2" : "=v"(dst) : "v"(addr), "i"(off))
#define DSR64(dst, addr, off) asm volatile("ds_read_b64 %0, %1 offset:%2" : "=v"(dst) : "v"(addr), "i"(off))
#define DSRTR(dst, addr, off) asm volatile("ds_read_b64_tr_b16 %0, %1 offset:%2" : "=v"(dst) : "v"(addr), "i"(off))
template <int N> __device__ __forceinline__ void wait_lgkm() { asm volatile("s_waitcnt lgkmcnt(%0)" ::"n"(N > 15 ? 15 : N) : "memory"); }
template <int N> __device__ __forceinline__ void wait_vm() { asm volatile("s_waitcnt vmcnt(%0)" ::"n"(N) : "memory"); }

constexpr int S1_A = 0, S1_G = 32768, S1_U = 51200, S1_E = 69632, S1_STG = 75776;

typedef float f32x2 __attribute__((ext_vector_type(2)));
typedef __bf16 bf16x2v __attribute__((ext_vector_type(2)));
__device__ __forceinline__ unsigned cvt_pk(float lo, float hi) { f32x2 v = {lo, hi}; return __builtin_bit_cast(unsigned, __builtin_convertvector(v, bf16x2v)); }
__device__ __forceinline__ float lo_f(unsigned p) { return __uint_as_float(p << 16); }
__device__ __forceinline__ float hi_f(unsigned p) { return __uint_as_float(p & 0xffff0000u); }
__device__ __forceinline__ unsigned glu_pair(float g0, float g1, float u0, float u1) {
    const unsigned gp = cvt_pk(g0, g1), up = cvt_pk(u0, u1);
    const float e0 = __builtin_amdgcn_exp2f(lo_f(gp) * -1.4426950408889634f), e1 = __builtin_amdgcn_exp2f(hi_f(gp) * -1.4426950408889634f);
    const unsigned sp = cvt_pk(__builtin_amdgcn_rcpf(1.0f + e0), __builtin_amdgcn_rcpf(1.0f + e1));
    return cvt_pk(lo_f(sp) * lo_f(up), hi_f(sp) * hi_f(up));
}

template <int NF, int NE> struct S1Sched {
    static constexpr int NG = 2 * NF + NE;
    static constexpr int ninst(int i) { return 2; }
    static constexpr int younger(int i) {
        int n = 0;
        for (int g = (i == 0 ? 0 : i - 1); g <= i; ++g) {
            if (i >= 2 && g == i - 2) continue;
            if (g == 2) n += 4;
            if (g + 2 < 2 * NG) n += ninst(g + 2);
        }
        if (i == 0) n += ninst(1);
        return n;
    }
};

template <int NF, int NE, int F0T, int E0T, bool HAS_E>
__device__ __forceinline__ void s1_body(char* shm, int lane, int wm,
                                        const bf16_t* xn, const bf16_t* wg, const bf16_t* wu, const bf16_t* we,
                                        int wid, int m0, int f0, int e0, bf16_t* hbuf, bf16_t* abT, const char* pf) {
    constexpr int NT = D_ / 64;
    asm volatile("" : "+v"(lane));
    const int srow = lane >> 3, schunk = (lane & 7) ^ srow;
    const unsigned a_off = (unsigned)(((m0 + wid * 32 + srow) * D_ + schunk * 8) * 2);
    unsigned b_off[5];
#pragma unroll
    for (int j = 0; j < 5; ++j) {
        int i = wid + 8 * j; if (i > 35) i = 35;
        const int ii = i < 18 ? i : i - 18;
        const int P = ii * 64 + lane, k = P / 18, c16p = P % 18, c32p = c16p >> 1;
        const int c32 = (c32p + 9 - 4 * ((k >> 3) & 1)) % 9;
        b_off[j] = (unsigned)((k * F_ + f0 + (2 * c32 + (c16p & 1)) * 8) * 2);
    }
    const unsigned e_off = (unsigned)((((e0 < D_ ? e0 : e0 - D_) + (wid < 6 ? wid : 5) * 8 + srow) * D_ + schunk * 8) * 2);
    constexpr int NEE = HAS_E ? NE : 0;
    using SC = S1Sched<NF, NEE>;
    constexpr int NG = SC::NG;
    const int fr = lane & 15, fq = lane >> 4, xr7 = lane & 7;
    const int g = lane >> 4, q = (lane & 15) >> 2, p = lane & 3;
    const unsigned lbase = (unsigned)(size_t)shm;
    const unsigned co0 = (unsigned)((fq ^ xr7) * 16);
    const unsigned a_ad = lbase + S1_A + (wm * 64 + fr) * 128 + co0;
    const unsigned e_ad = lbase + S1_E + fr * 128 + co0;
    const unsigned rb = lbase + S1_G + (8 * g + q) * 288 + p * 8;
    const unsigned bx_ad = rb + (g & 1) * 128, by_ad = rb - (g & 1) * 160;
    auto gl = [&](int j, int buf, int kt) {
        char* base = shm + buf * S1_STG;
        if (j < 4) glds16((const char*)(xn + j * 8 * D_ + kt * 64) + a_off, base + S1_A + (wid * 4 + j) * 1024);
        else if (j < 9) { int i = wid + 8 * (j - 4); if (i > 35) i = 35;
            glds16((const char*)((i < 18 ? wg : wu) + kt * 64 * F_) + b_off[j - 4], base + S1_G + i * 1024); }
        else if (HAS_E) glds16((const char*)(we + kt * 64) + e_off, base + S1_E + (wid < 6 ? wid : 5) * 1024);
    };
    f32x4 accg[NF][4] = {}, accu[NF][4] = {}, acce[NE][4] = {};
    bf16x8 af[2][4];
    s16x4 lo[3], hi[3];
#pragma unroll
    for (int j = 0; j < 10; ++j) gl(j, 0, 0);
#define S1_RD(i, sl) do { constexpr int gi_ = (i) % NG, ks_ = (i) / NG, w_ = (i) % 3; \
        if constexpr (gi_ < 2 * NF) { constexpr int c_ = F0T + gi_ / 2, off_ = (gi_ & 1) * 18432 + ks_ * 9216 + c_ * 32; \
            if constexpr (c_ < 5) { DSRTR(lo[w_], bx_ad + (sl), off_); DSRTR(hi[w_], bx_ad + (sl), off_ + 1152); } \
            else { DSRTR(lo[w_], by_ad + (sl), off_); DSRTR(hi[w_], by_ad + (sl), off_ + 1152); } } \
        else { DSR64(lo[w_], (e_ad + (sl)) ^ (ks_ * 64), (E0T + gi_ - 2 * NF) * 2048); DSR64(hi[w_], (e_ad + (sl)) ^ (ks_ * 64), (E0T + gi_ - 2 * NF) * 2048 + 8); } } while (0)
#pragma unroll 1
    for (int t = 0; t < NT; ++t) {
        const unsigned sl = (unsigned)(t & 1) * S1_STG;
        if (t == 0) wait_vm<0>(); else wait_vm<1>();
        __builtin_amdgcn_s_barrier(); asm volatile("" ::: "memory");
        const bool more = t + 1 < NT;
#pragma unroll
        for (int mt = 0; mt < 4; ++mt) DSR128(af[0][mt], a_ad + sl, mt * 2048);
        S1_RD(0, sl); S1_RD(1, sl);
        SB0();
#define S1_GROUP(i) do { constexpr int gi = (i) % NG, ks = (i) / NG, w = (i) % 3; \
            if constexpr ((i) == 2) { _Pragma("unroll") for (int mt = 0; mt < 4; ++mt) DSR128(af[1][mt], (a_ad + sl) ^ 64, mt * 2048); } \
            if constexpr ((i) + 2 < 2 * NG) S1_RD((i) + 2, sl); \
            if constexpr ((i) % 2 == 0 && (i) / 2 < 10) { if (more) gl((i) / 2, (t + 1) & 1, t + 1); } \
            if constexpr ((i) == 2 * NG - 2) { if (more) glds16(pf + (wid * 8 + t) * 1024 + lane * 16, shm + 2 * S1_STG + wid * 1024); } \
            wait_lgkm<SC::younger(i)>(); SB0(); \
            if constexpr (gi < 2 * NF) { const bf16x8 bf = __builtin_shufflevector(lo[w], hi[w], 0, 1, 2, 3, 4, 5, 6, 7); \
                _Pragma("unroll") for (int mt = 0; mt < 4; ++mt) { \
                    if constexpr ((gi & 1) == 0) accg[gi / 2][mt] = __builtin_amdgcn_mfma_f32_16x16x32_bf16(bf, af[ks][mt], accg[gi / 2][mt], 0, 0, 0); \
                    else                         accu[gi / 2][mt] = __builtin_amdgcn_mfma_f32_16x16x32_bf16(bf, af[ks][mt], accu[gi / 2][mt], 0, 0, 0); } } \
            else { const bf16x8 bf = __builtin_shufflevector(lo[w], hi[w], 0, 1, 2, 3, 4, 5, 6, 7); \
                _Pragma("unroll") for (int mt = 0; mt < 4; ++mt) \
                    acce[gi - 2 * NF][mt] = __builtin_amdgcn_mfma_f32_16x16x32_bf16(af[ks][mt], bf, acce[gi - 2 * NF][mt], 0, 0, 0); } \
            SB0(); } while (0)
        S1_GROUP(0); S1_GROUP(1); S1_GROUP(2); S1_GROUP(3); S1_GROUP(4); S1_GROUP(5); S1_GROUP(6); S1_GROUP(7);
        S1_GROUP(8); S1_GROUP(9); S1_GROUP(10); S1_GROUP(11); S1_GROUP(12); S1_GROUP(13); S1_GROUP(14); S1_GROUP(15);
        if constexpr (2 * NG > 16) { S1_GROUP(16); S1_GROUP(17); }
        if constexpr (2 * NG > 18) { S1_GROUP(18); S1_GROUP(19); }
        if constexpr (2 * NG > 20) { S1_GROUP(20); S1_GROUP(21); }
    }
    int lz = lane; asm volatile("" : "+v"(lz));
    const int fr2 = lz & 15, fq2 = lz >> 4;
#pragma unroll
    for (int mt = 0; mt < 4; ++mt)
#pragma unroll
        for (int n = 0; n < NF; ++n) {
            u32x2 o;
            o[0] = glu_pair(accg[n][mt][0], accg[n][mt][1], accu[n][mt][0], accu[n][mt][1]);
            o[1] = glu_pair(accg[n][mt][2], accg[n][mt][3], accu[n][mt][2], accu[n][mt][3]);
            const int tok = m0 + wm * 64 + mt * 16 + fr2, f = f0 + (F0T + n) * 16 + fq2 * 4;
            *(u32x2*)(hbuf + (size_t)tok * F_ + f) = o;
        }
    if constexpr (HAS_E) {
#pragma unroll
        for (int mt = 0; mt < 4; ++mt)
#pragma unroll
            for (int n = 0; n < NE; ++n) {
                u32x2 o; o[0] = pack2(acce[n][mt][0], acce[n][mt][1]); o[1] = pack2(acce[n][mt][2], acce[n][mt][3]);
                const int e = e0 + (E0T + n) * 16 + fr2, tok = m0 + wm * 64 + mt * 16 + fq2 * 4;
                *(u32x2*)(abT + (size_t)e * S_ + tok) = o;
            }
    }
    wait_vm<0>();
}

__global__ __launch_bounds__(512, 2) void stage1_kernel(const bf16_t* __restrict__ xn, const bf16_t* __restrict__ wg,
                                                        const bf16_t* __restrict__ wu, const bf16_t* __restrict__ wre,
                                                        const bf16_t* __restrict__ wim, bf16_t* __restrict__ hbuf,
                                                        bf16_t* __restrict__ abT, const bf16_t* __restrict__ cre,
                                                        const bf16_t* __restrict__ cim) {
    __shared__ __attribute__((aligned(1024))) char shm[2 * S1_STG + 8192];
    const int tid = threadIdx.x, wid = __builtin_amdgcn_readfirstlane(tid >> 6), lane = tid & 63;
    const int xcd = blockIdx.x & 7, idx = blockIdx.x >> 3;
    const int fg = xcd * 4 + (idx >> 3), m0 = (idx & 7) * 256, f0 = fg * 144;
    const bool has_dft = (fg & 3) != 3;
    const int e0 = ((fg >> 2) * 3 + (fg & 3)) * 48;
    const bf16_t* we = e0 < D_ ? wre : wim;
    const char* pf = (const char*)(blockIdx.x < 128 ? cre : cim) + (size_t)(blockIdx.x & 127) * 65536;
    const int wm = wid & 3;
    if (has_dft) {
        if (wid < 4) s1_body<5, 1, 0, 0, true>(shm, lane, wm, xn, wg, wu, we, wid, m0, f0, e0, hbuf, abT, pf);
        else         s1_body<4, 2, 5, 1, true>(shm, lane, wm, xn, wg, wu, we, wid, m0, f0, e0, hbuf, abT, pf);
    } else {
        if (wid < 4) s1_body<5, 1, 0, 0, false>(shm, lane, wm, xn, wg, wu, we, wid, m0, f0, e0, hbuf, abT, pf);
        else         s1_body<4, 2, 5, 1, false>(shm, lane, wm, xn, wg, wu, we, wid, m0, f0, e0, hbuf, abT, pf);
    }
}

constexpr int S2_STG = 16384 + 18432;

template <bool NN, bool DO_READ, bool BAR, int KSN>
__device__ __forceinline__ void s2_half(f32x4 (&acc)[9][2], bf16x8 (&a)[2], bf16x8 (&b)[9], s16x4 (&lo)[9], s16x4 (&hi)[9],
                                        unsigned aa, unsigned bb, unsigned bx, unsigned by) {
#pragma unroll
    for (int j = 0; j < 18; ++j) {
        const int n = j % 9, mt = j / 9;
        if (j < 9) { if constexpr (NN) { if (j == 0) wait_lgkm<17>(); else if (j == 1) wait_lgkm<15>(); else if (j == 2) wait_lgkm<13>(); else if (j == 3) wait_lgkm<11>();
                                         else if (j == 4) wait_lgkm<9>(); else if (j == 5) wait_lgkm<7>(); else if (j == 6) wait_lgkm<5>(); else if (j == 7) wait_lgkm<3>(); else wait_lgkm<1>(); }
                     else { if (j == 0) wait_lgkm<9>(); else if (j == 1) wait_lgkm<8>(); else if (j == 2) wait_lgkm<7>(); else if (j == 3) wait_lgkm<6>();
                            else if (j == 4) wait_lgkm<5>(); else if (j == 5) wait_lgkm<4>(); else if (j == 6) wait_lgkm<3>(); else if (j == 7) wait_lgkm<2>(); else wait_lgkm<1>(); } }
        else if (j == 9) wait_lgkm<0>();
        SB0();
        bf16x8 bf;
        if constexpr (NN) bf = __builtin_shufflevector(lo[n], hi[n], 0, 1, 2, 3, 4, 5, 6, 7); else bf = b[n];
        acc[n][mt] = __builtin_amdgcn_mfma_f32_16x16x32_bf16(bf, a[mt], acc[n][mt], 0, 0, 0);
        SB0();
        if (j == 9 && BAR) { __builtin_amdgcn_s_barrier(); asm volatile("" ::: "memory"); }
        if constexpr (DO_READ) {
            if (j == 9) DSR128(a[0], aa, 0);
            if (j >= 9) {
                if constexpr (!NN) DSR128(b[n], bb, 16384 + n * 2048);
                else {
                    if (n < 5) { DSRTR(lo[n], bx, 16384 + KSN * 9216 + n * 32); DSRTR(hi[n], bx, 16384 + KSN * 9216 + n * 32 + 1152); }
                    else       { DSRTR(lo[n], by, 16384 + KSN * 9216 + n * 32); DSRTR(hi[n], by, 16384 + KSN * 9216 + n * 32 + 1152); }
                }
            }
            if (j == 17) DSR128(a[1], aa, 2048);
        }
        SB0();
    }
}
template <bool NN>
__device__ __forceinline__ void s2_compute(f32x4 (&acc)[9][2], int nkt, unsigned a_ad, unsigned bn_ad, unsigned bx_ad, unsigned by_ad) {
    bf16x8 a[2], b[9]; s16x4 lo[9], hi[9];
    __builtin_amdgcn_s_barrier(); asm volatile("" ::: "memory");
    DSR128(a[0], a_ad, 0);
#pragma unroll
    for (int n = 0; n < 9; ++n) {
        if constexpr (!NN) DSR128(b[n], bn_ad, 16384 + n * 2048);
        else {
            if (n < 5) { DSRTR(lo[n], bx_ad, 16384 + n * 32); DSRTR(hi[n], bx_ad, 16384 + n * 32 + 1152); }
            else       { DSRTR(lo[n], by_ad, 16384 + n * 32); DSRTR(hi[n], by_ad, 16384 + n * 32 + 1152); }
        }
    }
    DSR128(a[1], a_ad, 2048);
    SB0();
#pragma unroll 1
    for (int t = 0; t < nkt; ++t) {
        const unsigned sn = (unsigned)((t + 1) & 3) * S2_STG;
        s2_half<NN, true, true, 0>(acc, a, b, lo, hi, a_ad + sn, bn_ad + sn, bx_ad + sn, by_ad + sn);
    }
    LGKM0(); SB0();
}

#define S2_NL 8
constexpr int S2_G = (34 + S2_NL - 1) / S2_NL;

__global__ __launch_bounds__((8 + S2_NL) * 64, (8 + S2_NL) / 4) void stage2_kernel(const bf16_t* __restrict__ cre, const bf16_t* __restrict__ cim,
                                                        const bf16_t* __restrict__ abT, const bf16_t* __restrict__ hbuf,
                                                        const bf16_t* __restrict__ wd, float* pbuf, unsigned* cnt,
                                                        const bf16_t* __restrict__ x, bf16_t* __restrict__ out) {
    constexpr int STG = S2_STG;
    __shared__ __attribute__((aligned(1024))) char shm[4 * STG];
    const int tid = threadIdx.x, widx = __builtin_amdgcn_readfirstlane(tid >> 6), lane = tid & 63;
    const int xcd = blockIdx.x & 7, idx = blockIdx.x >> 3;
    const int ty = xcd & 3, m0 = ((xcd >> 2) * 8 + (idx >> 2)) * 128, n0 = (idx & 3) * 144;
    const int fr = lane & 15, fq = lane >> 4, xr7 = lane & 7;
    const bool nn = ty >= 2;
    const int nkt = nn ? 36 : 32;
    if (widx >= 8) {
        const int lw = widx - 8;
        const int srow = lane >> 3, schunk = (lane & 7) ^ srow;
        const int lda = nn ? F_ : S_;
        const bf16_t* abase = ty == 0 ? cre : (ty == 1 ? cim : (ty == 2 ? hbuf : hbuf + 2304));
        const bf16_t* src[S2_G]; size_t step[S2_G]; int dst[S2_G];
#pragma unroll
        for (int j = 0; j < S2_G; ++j) {
            int i = lw + S2_NL * j; if (i > 33) i = 33;
            dst[j] = i * 1024;
            if (i < 16) { src[j] = abase + (size_t)(m0 + i * 8 + srow) * lda + schunk * 8; step[j] = 64; }
            else if (!nn) { src[j] = abT + (size_t)(ty * D_ + n0 + (i - 16) * 8 + srow) * S_ + schunk * 8; step[j] = 64; }
            else { src[j] = wd + (size_t)(n0 + (i - 16) * 8 + srow) * F_ + (ty - 2) * 2304 + schunk * 8; step[j] = 64; }
        }
        auto stage = [&](int buf, int kt) {
            char* base = shm + buf * STG;
#pragma unroll
            for (int j = 0; j < S2_G; ++j) glds16(src[j] + (size_t)kt * step[j], base + dst[j]);
        };
        stage(0, 0); stage(1, 1); stage(2, 2);
        wait_vm<2 * S2_G>(); __builtin_amdgcn_s_barrier(); asm volatile("" ::: "memory");
#pragma unroll 1
        for (int t = 0; t < nkt; ++t) {
            if (t + 3 < nkt) { stage((t + 3) & 3, t + 3); wait_vm<2 * S2_G>(); }
            else if (t + 2 < nkt) wait_vm<S2_G>();
            else wait_vm<0>();
            __builtin_amdgcn_s_barrier(); asm volatile("" ::: "memory");
        }
        return;
    }
    const int wid = widx & 3, kh = widx >> 2;
    const int g = lane >> 4, q = (lane & 15) >> 2, p = lane & 3;
    const unsigned lbase = (unsigned)(size_t)shm;
    const unsigned co0 = (unsigned)(((fq ^ xr7) * 16) ^ (kh * 64));
    const unsigned a_ad = lbase + (wid * 32 + fr) * 128 + co0;
    const unsigned bn_ad = lbase + fr * 128 + co0;
    const unsigned rb = lbase + (8 * g + q) * 288 + p * 8 + kh * 9216;
    const unsigned bx_ad = rb + (g & 1) * 128, by_ad = rb - (g & 1) * 160;
    f32x4 acc[9][2] = {};
    s2_compute<false>(acc, nkt, a_ad, bn_ad, bx_ad, by_ad);
    __builtin_amdgcn_s_barrier(); asm volatile("" ::: "memory");
    f32x4* red = (f32x4*)shm + wid * 18 * 64 + lane;
    if (kh == 1) {
#pragma unroll
        for (int i = 0; i < 18; ++i) red[i * 64] = acc[i >> 1][i & 1];
    }
    __syncthreads();
    int lz = lane; asm volatile("" : "+v"(lz));
    const __amdgpu_buffer_rsrc_t prs = __builtin_amdgcn_make_buffer_rsrc(pbuf, 0, 4u * S_ * D_ * 4u, 0x00020000);
    if (kh == 0) {
        const int fr2 = lz & 15, fq2 = lz >> 4;
#pragma unroll
        for (int mt = 0; mt < 2; ++mt)
#pragma unroll
            for (int n = 0; n < 9; ++n) {
                const int tok = m0 + wid * 32 + mt * 16 + fr2, d = n0 + n * 16 + fq2 * 4;
                const f32x4 v = acc[n][mt] + red[(n * 2 + mt) * 64];
                __builtin_amdgcn_raw_buffer_store_b128(__builtin_bit_cast(u32x4, v), prs, (unsigned)(((ty * S_ + tok) * D_ + d) * 4), 0, 16);
            }
        asm volatile("s_waitcnt vmcnt(0)" ::: "memory");
    }
    __syncthreads();
    const int tile = (m0 >> 7) * 4 + (idx & 3);
    if (tid == 0) {
        __hip_atomic_fetch_add(cnt + tile * 32, 1u, __ATOMIC_RELAXED, __HIP_MEMORY_SCOPE_AGENT);
        for (unsigned spins = 0; spins < (1u << 22); ++spins) {
            if (__hip_atomic_load(cnt + tile * 32, __ATOMIC_RELAXED, __HIP_MEMORY_SCOPE_AGENT) >= 4u) break;
            __builtin_amdgcn_s_sleep(2);
        }
    }
    __syncthreads();
    for (int i = tid; i < 32 * 36; i += 512) {
        const int tok = m0 + ty * 32 + i / 36, d = n0 + (i % 36) * 4;
        const unsigned o = (unsigned)((tok * D_ + d) * 4);
        f32x4 pv[4];
#pragma unroll
        for (int k = 0; k < 4; ++k) pv[k] = __builtin_bit_cast(f32x4, __builtin_amdgcn_raw_buffer_load_b128(prs, o + (unsigned)k * (S_ * D_ * 4u), 0, 16));
        const u32x2 xv = *(const u32x2*)(x + (size_t)tok * D_ + d);
        float r[4];
#pragma unroll
        for (int j = 0; j < 4; ++j) {
            const float sv = rbf(rbf(pv[0][j]) - rbf(pv[1][j]));
            const float dv = rbf(pv[2][j] + pv[3][j]);
            const float sl = rbf(dv * __builtin_amdgcn_rcpf(1.0f + __expf(-dv)));
            const float xf = (j & 1) ? hi_f(xv[j >> 1]) : lo_f(xv[j >> 1]);
            r[j] = xf + rbf(sv * sl);
        }
        u32x2 ov; ov[0] = cvt_pk(r[0], r[1]); ov[1] = cvt_pk(r[2], r[3]);
        *(u32x2*)(out + (size_t)tok * D_ + d) = ov;
    }
}

extern "C" void kernel_launch(void* const* d_in, const int* in_sizes, int n_in,
                              void* d_out, int out_size, void* d_ws, size_t ws_size,
                              hipStream_t stream) {
    const bf16_t* x = (const bf16_t*)d_in[0];
    const bf16_t* cre = (const bf16_t*)d_in[1];
    const bf16_t* cim = (const bf16_t*)d_in[2];
    const bf16_t* wre = (const bf16_t*)d_in[3];
    const bf16_t* wim = (const bf16_t*)d_in[4];
    const bf16_t* wg = (const bf16_t*)d_in[6];
    const bf16_t* wu = (const bf16_t*)d_in[7];
    const bf16_t* wd = (const bf16_t*)d_in[8];
    bf16_t* out = (bf16_t*)d_out;
    char* ws = (char*)d_ws;
    bf16_t* xn = (bf16_t*)ws;                       ws += (size_t)S_ * D_ * 2;
    bf16_t* abT = (bf16_t*)ws;                      ws += (size_t)2 * D_ * S_ * 2;
    bf16_t* hbuf = (bf16_t*)ws;                     ws += (size_t)S_ * F_ * 2;
    float* pbuf = (float*)ws;                       ws += (size_t)4 * S_ * D_ * 4;
    bf16_t* wdT = (bf16_t*)ws;                      ws += (size_t)D_ * F_ * 2;
    unsigned* cnt = (unsigned*)ws;                  ws += 64 * 32 * 4;
    ln_kernel<<<PREP_LN + PREP_TR + 2 * PREP_PFW + 2 * PREP_PFH, 256, 0, stream>>>(x, xn, wg, wu, wre, wim, wd, wdT, cnt);
    stage1_kernel<<<256, 512, 0, stream>>>(xn, wg, wu, wre, wim, hbuf, abT, cre, cim);
    stage2_kernel<<<256, (8 + S2_NL) * 64, 0, stream>>>(cre, cim, abT, hbuf, wdT, pbuf, cnt, x, out);
}
```

```cpp
#include <hip/hip_runtime.h>
#include <hip/hip_bf16.h>

typedef unsigned short bf16_t;
typedef short bf16x8 __attribute__((ext_vector_type(8)));
typedef short s16x4 __attribute__((ext_vector_type(4)));
typedef short v4i16_t __attribute__((ext_vector_type(4)));
typedef float f32x4 __attribute__((ext_vector_type(4)));
typedef unsigned u32x2 __attribute__((ext_vector_type(2)));
typedef unsigned u32x4 __attribute__((ext_vector_type(4)));

#define LAS __attribute__((address_space(3)))

constexpr int S_ = 2048, D_ = 576, F_ = 4608;
#define PHASE_MODE 1

__device__ __forceinline__ float bf2f(unsigned short u) { return __uint_as_float(((unsigned)u) << 16); }
__device__ __forceinline__ unsigned short f2bf(float f) { return __builtin_bit_cast(unsigned short, (__bf16)f); }
__device__ __forceinline__ float rbf(float f) { return bf2f(f2bf(f)); }
__device__ __forceinline__ unsigned pack2(float lo, float hi) { return (unsigned)f2bf(lo) | ((unsigned)f2bf(hi) << 16); }

__device__ __forceinline__ s16x4 tr_read(const char* p) {
    return __builtin_bit_cast(s16x4, __builtin_amdgcn_ds_read_tr16_b64_v4i16((LAS v4i16_t*)p));
}
__device__ __forceinline__ bf16x8 tr_frag(const char* p0, const char* p1) {
    s16x4 a = tr_read(p0), b = tr_read(p1);
    bf16x8 r;
    r[0] = a[0]; r[1] = a[1]; r[2] = a[2]; r[3] = a[3];
    r[4] = b[0]; r[5] = b[1]; r[6] = b[2]; r[7] = b[3];
    return r;
}
__device__ __forceinline__ void glds16(const void* g, char* l) {
    __builtin_amdgcn_global_load_lds((const unsigned*)g, (LAS unsigned*)l, 16, 0, 0);
}
#define WAIT_V0() asm volatile("s_waitcnt vmcnt(0)" ::: "memory")

constexpr int PREP_LN = S_ / 4, PREP_TR = (F_ / 64) * (D_ / 64), PREP_PFW = D_ * F_ * 2 / 32768, PREP_PFH = (D_ * D_ * 2 + 32767) / 32768;
__global__ __launch_bounds__(256) void ln_kernel(const bf16_t* __restrict__ x, bf16_t* __restrict__ xn,
                                                 const bf16_t* __restrict__ p0, const bf16_t* __restrict__ p1,
                                                 const bf16_t* __restrict__ p2, const bf16_t* __restrict__ p3,
                                                 const bf16_t* __restrict__ wd, bf16_t* __restrict__ wdT, unsigned* __restrict__ cnt) {
    __shared__ unsigned short tile[64][66];
    if (blockIdx.x == 0) { for (int i = threadIdx.x; i < 64 * 32; i += 256) cnt[i] = 0u; }
    if (blockIdx.x >= PREP_LN + PREP_TR) {
        int bb = blockIdx.x - PREP_LN - PREP_TR;
        const char* base; size_t lim;
        if (bb < PREP_PFW) { base = (const char*)p0; lim = (size_t)D_ * F_ * 2; }
        else if ((bb -= PREP_PFW) < PREP_PFW) { base = (const char*)p1; lim = (size_t)D_ * F_ * 2; }
        else if ((bb -= PREP_PFW) < PREP_PFH) { base = (const char*)p2; lim = (size_t)D_ * D_ * 2; }
        else { bb -= PREP_PFH; base = (const char*)p3; lim = (size_t)D_ * D_ * 2; }
        u32x4 v[8];
#pragma unroll
        for (int i = 0; i < 8; ++i) {
            size_t o = (size_t)bb * 32768 + i * 4096 + threadIdx.x * 16;
            if (o >= lim) o = 0;
            v[i] = *(const u32x4*)(base + o);
        }
#pragma unroll
        for (int i = 0; i < 8; ++i) asm volatile("" ::"v"(v[i]));
        return;
    }
    if (blockIdx.x >= PREP_LN) {
        const int b = blockIdx.x - PREP_LN, f0 = (b / 9) * 64, d0 = (b % 9) * 64;
        const int r = threadIdx.x >> 3, c = (threadIdx.x & 7) * 8;
#pragma unroll
        for (int h = 0; h < 2; ++h) {
            const bf16x8 v = *(const bf16x8*)(wd + (size_t)(f0 + r + 32 * h) * D_ + d0 + c);
#pragma unroll
            for (int j = 0; j < 8; ++j) tile[r + 32 * h][c + j] = (unsigned short)v[j];
        }
        __syncthreads();
#pragma unroll
        for (int h = 0; h < 2; ++h) {
            bf16x8 v;
#pragma unroll
            for (int j = 0; j < 8; ++j) v[j] = (short)tile[c + j][r + 32 * h];
            *(bf16x8*)(wdT + (size_t)(d0 + r + 32 * h) * F_ + f0 + c) = v;
        }
        return;
    }
    const int row = blockIdx.x * 4 + (threadIdx.x >> 6), lane = threadIdx.x & 63;
    const bf16_t* xr = x + (size_t)row * D_;
    bf16x8 v0 = *(const bf16x8*)(xr + lane * 8);
    bf16x8 v1 = {0, 0, 0, 0, 0, 0, 0, 0};
    const bool has2 = lane < 8;
    if (has2) v1 = *(const bf16x8*)(xr + 512 + lane * 8);
    float f[16];
    float s = 0.f;
#pragma unroll
    for (int j = 0; j < 8; ++j) { f[j] = bf2f((unsigned short)v0[j]); f[8 + j] = bf2f((unsigned short)v1[j]); s += f[j] + f[8 + j]; }
#pragma unroll
    for (int o = 32; o > 0; o >>= 1) s += __shfl_xor(s, o);
    const float mean = s * (1.0f / D_);
    float q = 0.f;
#pragma unroll
    for (int j = 0; j < 8; ++j) { float d0 = f[j] - mean; q += d0 * d0; if (has2) { float d1 = f[8 + j] - mean; q += d1 * d1; } }
#pragma unroll
    for (int o = 32; o > 0; o >>= 1) q += __shfl_xor(q, o);
    const float var = q * (1.0f / D_);
    const float mb = rbf(mean), vb = rbf(var);
    const float den = rbf(sqrtf(rbf(vb + 1e-5f)));
    bf16x8 o0, o1;
#pragma unroll
    for (int j = 0; j < 8; ++j) {
        o0[j] = (short)f2bf(rbf(f[j] - mb) / den);
        o1[j] = (short)f2bf(rbf(f[8 + j] - mb) / den);
    }
    bf16_t* orow = xn + (size_t)row * D_;
    *(bf16x8*)(orow + lane * 8) = o0;
    if (has2) *(bf16x8*)(orow + 512 + lane * 8) = o1;
}

#define LGKM0() asm volatile("s_waitcnt lgkmcnt(0)" ::: "memory")
#define SB0() __builtin_amdgcn_sched_barrier(0)
#define DSR128(dst, addr, off) asm volatile("ds_read_b128 %0, %1 offset:%2" : "=v"(dst) : "v"(addr), "i"(off))
#define DSR64(dst, addr, off) asm volatile("ds_read_b64 %0, %1 offset:%2" : "=v"(dst) : "v"(addr), "i"(off))
#define DSRTR(dst, addr, off) asm volatile("ds_read_b64_tr_b16 %0, %1 offset:%2" : "=v"(dst) : "v"(addr), "i"(off))
template <int N> __device__ __forceinline__ void wait_lgkm() { asm volatile("s_waitcnt lgkmcnt(%0)" ::"n"(N > 15 ? 15 : N) : "memory"); }
template <int N> __device__ __forceinline__ void wait_vm() { asm volatile("s_waitcnt vmcnt(%0)" ::"n"(N) : "memory"); }

constexpr int S1_A = 0, S1_G = 32768, S1_U = 51200, S1_E = 69632, S1_STG = 75776;

typedef float f32x2 __attribute__((ext_vector_type(2)));
typedef __bf16 bf16x2v __attribute__((ext_vector_type(2)));
__device__ __forceinline__ unsigned cvt_pk(float lo, float hi) { f32x2 v = {lo, hi}; return __builtin_bit_cast(unsigned, __builtin_convertvector(v, bf16x2v)); }
__device__ __forceinline__ float lo_f(unsigned p) { return __uint_as_float(p << 16); }
__device__ __forceinline__ float hi_f(unsigned p) { return __uint_as_float(p & 0xffff0000u); }
__device__ __forceinline__ unsigned glu_pair(float g0, float g1, float u0, float u1) {
    const unsigned gp = cvt_pk(g0, g1), up = cvt_pk(u0, u1);
    const float e0 = __builtin_amdgcn_exp2f(lo_f(gp) * -1.4426950408889634f), e1 = __builtin_amdgcn_exp2f(hi_f(gp) * -1.4426950408889634f);
    const unsigned sp = cvt_pk(__builtin_amdgcn_rcpf(1.0f + e0), __builtin_amdgcn_rcpf(1.0f + e1));
    return cvt_pk(lo_f(sp) * lo_f(up), hi_f(sp) * hi_f(up));
}

template <int NF, int NE> struct S1Sched {
    static constexpr int NG = 2 * NF + NE;
    static constexpr int ninst(int i) { return 2; }
    static constexpr int younger(int i) {
        int n = 0;
        for (int g = (i == 0 ? 0 : i - 1); g <= i; ++g) {
            if (i >= 2 && g == i - 2) continue;
            if (g == 2) n += 4;
            if (g + 2 < 2 * NG) n += ninst(g + 2);
        }
        if (i == 0) n += ninst(1);
        return n;
    }
};

template <int NF, int NE, int F0T, int E0T, bool HAS_E>
__device__ __forceinline__ void s1_body(char* shm, int lane, int wm,
                                        const bf16_t* xn, const bf16_t* wg, const bf16_t* wu, const bf16_t* we,
                                        int wid, int m0, int f0, int e0, bf16_t* hbuf, bf16_t* abT, const char* pf) {
    constexpr int NT = D_ / 64;
    asm volatile("" : "+v"(lane));
    const int srow = lane >> 3, schunk = (lane & 7) ^ srow;
    const unsigned a_off = (unsigned)(((m0 + wid * 32 + srow) * D_ + schunk * 8) * 2);
    unsigned b_off[5];
#pragma unroll
    for (int j = 0; j < 5; ++j) {
        int i = wid + 8 * j; if (i > 35) i = 35;
        const int ii = i < 18 ? i : i - 18;
        const int P = ii * 64 + lane, k = P / 18, c16p = P % 18, c32p = c16p >> 1;
        const int c32 = (c32p + 9 - 4 * ((k >> 3) & 1)) % 9;
        b_off[j] = (unsigned)((k * F_ + f0 + (2 * c32 + (c16p & 1)) * 8) * 2);
    }
    const unsigned e_off = (unsigned)((((e0 < D_ ? e0 : e0 - D_) + (wid < 6 ? wid : 5) * 8 + srow) * D_ + schunk * 8) * 2);
    constexpr int NEE = HAS_E ? NE : 0;
    using SC = S1Sched<NF, NEE>;
    constexpr int NG = SC::NG;
    const int fr = lane & 15, fq = lane >> 4, xr7 = lane & 7;
    const int g = lane >> 4, q = (lane & 15) >> 2, p = lane & 3;
    const unsigned lbase = (unsigned)(size_t)shm;
    const unsigned co0 = (unsigned)((fq ^ xr7) * 16);
    const unsigned a_ad = lbase + S1_A + (wm * 64 + fr) * 128 + co0;
    const unsigned e_ad = lbase + S1_E + fr * 128 + co0;
    const unsigned rb = lbase + S1_G + (8 * g + q) * 288 + p * 8;
    const unsigned bx_ad = rb + (g & 1) * 128, by_ad = rb - (g & 1) * 160;
    auto gl = [&](int j, int buf, int kt) {
        char* base = shm + buf * S1_STG;
        if (j < 4) glds16((const char*)(xn + j * 8 * D_ + kt * 64) + a_off, base + S1_A + (wid * 4 + j) * 1024);
        else if (j < 9) { int i = wid + 8 * (j - 4); if (i > 35) i = 35;
            glds16((const char*)((i < 18 ? wg : wu) + kt * 64 * F_) + b_off[j - 4], base + S1_G + i * 1024); }
        else if (HAS_E) glds16((const char*)(we + kt * 64) + e_off, base + S1_E + (wid < 6 ? wid : 5) * 1024);
    };
    f32x4 accg[NF][4] = {}, accu[NF][4] = {}, acce[NE][4] = {};
    bf16x8 af[2][4];
    s16x4 lo[3], hi[3];
#pragma unroll
    for (int j = 0; j < 10; ++j) gl(j, 0, 0);
#define S1_RD(i, sl) do { constexpr int gi_ = (i) % NG, ks_ = (i) / NG, w_ = (i) % 3; \
        if constexpr (gi_ < 2 * NF) { constexpr int c_ = F0T + gi_ / 2, off_ = (gi_ & 1) * 18432 + ks_ * 9216 + c_ * 32; \
            if constexpr (c_ < 5) { DSRTR(lo[w_], bx_ad + (sl), off_); DSRTR(hi[w_], bx_ad + (sl), off_ + 1152); } \
            else { DSRTR(lo[w_], by_ad + (sl), off_); DSRTR(hi[w_], by_ad + (sl), off_ + 1152); } } \
        else { DSR64(lo[w_], (e_ad + (sl)) ^ (ks_ * 64), (E0T + gi_ - 2 * NF) * 2048); DSR64(hi[w_], (e_ad + (sl)) ^ (ks_ * 64), (E0T + gi_ - 2 * NF) * 2048 + 8); } } while (0)
#pragma unroll 1
    for (int t = 0; t < NT; ++t) {
        const unsigned sl = (unsigned)(t & 1) * S1_STG;
        if (t == 0) wait_vm<0>(); else wait_vm<1>();
        __builtin_amdgcn_s_barrier(); asm volatile("" ::: "memory");
        const bool more = t + 1 < NT;
#pragma unroll
        for (int mt = 0; mt < 4; ++mt) DSR128(af[0][mt], a_ad + sl, mt * 2048);
        S1_RD(0, sl); S1_RD(1, sl);
        SB0();
#define S1_GROUP(i) do { constexpr int gi = (i) % NG, ks = (i) / NG, w = (i) % 3; \
            if constexpr ((i) == 2) { _Pragma("unroll") for (int mt = 0; mt < 4; ++mt) DSR128(af[1][mt], (a_ad + sl) ^ 64, mt * 2048); } \
            if constexpr ((i) + 2 < 2 * NG) S1_RD((i) + 2, sl); \
            if constexpr ((i) % 2 == 0 && (i) / 2 < 10) { if (more) gl((i) / 2, (t + 1) & 1, t + 1); } \
            if constexpr ((i) == 2 * NG - 2) { if (more) glds16(pf + (wid * 8 + t) * 1024 + lane * 16, shm + 2 * S1_STG + wid * 1024); } \
            wait_lgkm<SC::younger(i)>(); SB0(); \
            if constexpr (gi < 2 * NF) { const bf16x8 bf = __builtin_shufflevector(lo[w], hi[w], 0, 1, 2, 3, 4, 5, 6, 7); \
                _Pragma("unroll") for (int mt = 0; mt < 4; ++mt) { \
                    if constexpr ((gi & 1) == 0) accg[gi / 2][mt] = __builtin_amdgcn_mfma_f32_16x16x32_bf16(bf, af[ks][mt], accg[gi / 2][mt], 0, 0, 0); \
                    else                         accu[gi / 2][mt] = __builtin_amdgcn_mfma_f32_16x16x32_bf16(bf, af[ks][mt], accu[gi / 2][mt], 0, 0, 0); } } \
            else { const bf16x8 bf = __builtin_shufflevector(lo[w], hi[w], 0, 1, 2, 3, 4, 5, 6, 7); \
                _Pragma("unroll") for (int mt = 0; mt < 4; ++mt) \
                    acce[gi - 2 * NF][mt] = __builtin_amdgcn_mfma_f32_16x16x32_bf16(af[ks][mt], bf, acce[gi - 2 * NF][mt], 0, 0, 0); } \
            SB0(); } while (0)
        S1_GROUP(0); S1_GROUP(1); S1_GROUP(2); S1_GROUP(3); S1_GROUP(4); S1_GROUP(5); S1_GROUP(6); S1_GROUP(7);
        S1_GROUP(8); S1_GROUP(9); S1_GROUP(10); S1_GROUP(11); S1_GROUP(12); S1_GROUP(13); S1_GROUP(14); S1_GROUP(15);
        if constexpr (2 * NG > 16) { S1_GROUP(16); S1_GROUP(17); }
        if constexpr (2 * NG > 18) { S1_GROUP(18); S1_GROUP(19); }
        if constexpr (2 * NG > 20) { S1_GROUP(20); S1_GROUP(21); }
    }
    constexpr int HROW = 304, EROW = 528, EBASE = 256 * HROW;
    __builtin_amdgcn_s_barrier(); asm volatile("" ::: "memory");
    int lz = lane; asm volatile("" : "+v"(lz));
    const int fr2 = lz & 15, fq2 = lz >> 4;
#pragma unroll
    for (int mt = 0; mt < 4; ++mt)
#pragma unroll
        for (int n = 0; n < NF; ++n) {
            u32x2 o;
            o[0] = glu_pair(accg[n][mt][0], accg[n][mt][1], accu[n][mt][0], accu[n][mt][1]);
            o[1] = glu_pair(accg[n][mt][2], accg[n][mt][3], accu[n][mt][2], accu[n][mt][3]);
            *(u32x2*)(shm + (wm * 64 + mt * 16 + fr2) * HROW + ((F0T + n) * 16 + fq2 * 4) * 2) = o;
        }
    if constexpr (HAS_E) {
#pragma unroll
        for (int mt = 0; mt < 4; ++mt)
#pragma unroll
            for (int n = 0; n < NE; ++n) {
                u32x2 o; o[0] = cvt_pk(acce[n][mt][0], acce[n][mt][1]); o[1] = cvt_pk(acce[n][mt][2], acce[n][mt][3]);
                *(u32x2*)(shm + EBASE + ((E0T + n) * 16 + fr2) * EROW + (wm * 64 + mt * 16 + fq2 * 4) * 2) = o;
            }
    }
    asm volatile("s_waitcnt lgkmcnt(0)" ::: "memory");
    __builtin_amdgcn_s_barrier(); asm volatile("" ::: "memory");
    const int tz = wid * 64 + lz;
#pragma unroll
    for (int i = 0; i < 9; ++i) {
        const int c = tz + i * 512, row = c / 18, ch = c % 18;
        const u32x4 v = *(const u32x4*)(shm + row * HROW + ch * 16);
        *(u32x4*)(hbuf + (size_t)(m0 + row) * F_ + f0 + ch * 8) = v;
    }
    if constexpr (HAS_E) {
#pragma unroll
        for (int i = 0; i < 3; ++i) {
            const int c = tz + i * 512, row = c >> 5, ch = c & 31;
            const u32x4 v = *(const u32x4*)(shm + EBASE + row * EROW + ch * 16);
            *(u32x4*)(abT + (size_t)(e0 + row) * S_ + m0 + ch * 8) = v;
        }
    }
    wait_vm<0>();
}

__global__ __launch_bounds__(512, 2) void stage1_kernel(const bf16_t* __restrict__ xn, const bf16_t* __restrict__ wg,
                                                        const bf16_t* __restrict__ wu, const bf16_t* __restrict__ wre,
                                                        const bf16_t* __restrict__ wim, bf16_t* __restrict__ hbuf,
                                                        bf16_t* __restrict__ abT, const bf16_t* __restrict__ cre,
                                                        const bf16_t* __restrict__ cim) {
    __shared__ __attribute__((aligned(1024))) char shm[2 * S1_STG + 8192];
    const int tid = threadIdx.x, wid = __builtin_amdgcn_readfirstlane(tid >> 6), lane = tid & 63;
    const int xcd = blockIdx.x & 7, idx = blockIdx.x >> 3;
    const int fg = xcd * 4 + (idx >> 3), m0 = (idx & 7) * 256, f0 = fg * 144;
    const bool has_dft = (fg & 3) != 3;
    const int e0 = ((fg >> 2) * 3 + (fg & 3)) * 48;
    const bf16_t* we = e0 < D_ ? wre : wim;
    const char* pf = (const char*)(blockIdx.x < 128 ? cre : cim) + (size_t)(blockIdx.x & 127) * 65536;
    const int wm = wid & 3;
    if (has_dft) {
        if (wid < 4) s1_body<5, 1, 0, 0, true>(shm, lane, wm, xn, wg, wu, we, wid, m0, f0, e0, hbuf, abT, pf);
        else         s1_body<4, 2, 5, 1, true>(shm, lane, wm, xn, wg, wu, we, wid, m0, f0, e0, hbuf, abT, pf);
    } else {
        if (wid < 4) s1_body<5, 1, 0, 0, false>(shm, lane, wm, xn, wg, wu, we, wid, m0, f0, e0, hbuf, abT, pf);
        else         s1_body<4, 2, 5, 1, false>(shm, lane, wm, xn, wg, wu, we, wid, m0, f0, e0, hbuf, abT, pf);
    }
}

constexpr int S2_STG = 16384 + 18432;

template <bool NN, bool DO_READ, bool BAR, int KSN>
__device__ __forceinline__ void s2_half(f32x4 (&acc)[9][2], bf16x8 (&a)[2], bf16x8 (&b)[9], s16x4 (&lo)[9], s16x4 (&hi)[9],
                                        unsigned aa, unsigned bb, unsigned bx, unsigned by) {
#pragma unroll
    for (int j = 0; j < 18; ++j) {
        const int n = j % 9, mt = j / 9;
        if (j < 9) { if constexpr (NN) { if (j == 0) wait_lgkm<17>(); else if (j == 1) wait_lgkm<15>(); else if (j == 2) wait_lgkm<13>(); else if (j == 3) wait_lgkm<11>();
                                         else if (j == 4) wait_lgkm<9>(); else if (j == 5) wait_lgkm<7>(); else if (j == 6) wait_lgkm<5>(); else if (j == 7) wait_lgkm<3>(); else wait_lgkm<1>(); }
                     else { if (j == 0) wait_lgkm<9>(); else if (j == 1) wait_lgkm<8>(); else if (j == 2) wait_lgkm<7>(); else if (j == 3) wait_lgkm<6>();
                            else if (j == 4) wait_lgkm<5>(); else if (j == 5) wait_lgkm<4>(); else if (j == 6) wait_lgkm<3>(); else if (j == 7) wait_lgkm<2>(); else wait_lgkm<1>(); } }
        else if (j == 9) wait_lgkm<0>();
        SB0();
        bf16x8 bf;
        if constexpr (NN) bf = __builtin_shufflevector(lo[n], hi[n], 0, 1, 2, 3, 4, 5, 6, 7); else bf = b[n];
        acc[n][mt] = __builtin_amdgcn_mfma_f32_16x16x32_bf16(bf, a[mt], acc[n][mt], 0, 0, 0);
        SB0();
        if (j == 9 && BAR) { __builtin_amdgcn_s_barrier(); asm volatile("" ::: "memory"); }
        if constexpr (DO_READ) {
            if (j == 9) DSR128(a[0], aa, 0);
            if (j >= 9) {
                if constexpr (!NN) DSR128(b[n], bb, 16384 + n * 2048);
                else {
                    if (n < 5) { DSRTR(lo[n], bx, 16384 + KSN * 9216 + n * 32); DSRTR(hi[n], bx, 16384 + KSN * 9216 + n * 32 + 1152); }
                    else       { DSRTR(lo[n], by, 16384 + KSN * 9216 + n * 32); DSRTR(hi[n], by, 16384 + KSN * 9216 + n * 32 + 1152); }
                }
            }
            if (j == 17) DSR128(a[1], aa, 2048);
        }
        SB0();
    }
}
template <bool NN>
__device__ __forceinline__ void s2_compute(f32x4 (&acc)[9][2], int nkt, unsigned a_ad, unsigned bn_ad, unsigned bx_ad, unsigned by_ad) {
    bf16x8 a[2], b[9]; s16x4 lo[9], hi[9];
    __builtin_amdgcn_s_barrier(); asm volatile("" ::: "memory");
    DSR128(a[0], a_ad, 0);
#pragma unroll
    for (int n = 0; n < 9; ++n) {
        if constexpr (!NN) DSR128(b[n], bn_ad, 16384 + n * 2048);
        else {
            if (n < 5) { DSRTR(lo[n], bx_ad, 16384 + n * 32); DSRTR(hi[n], bx_ad, 16384 + n * 32 + 1152); }
            else       { DSRTR(lo[n], by_ad, 16384 + n * 32); DSRTR(hi[n], by_ad, 16384 + n * 32 + 1152); }
        }
    }
    DSR128(a[1], a_ad, 2048);
    SB0();
#pragma unroll 1
    for (int t = 0; t < nkt; ++t) {
        const unsigned sn = (unsigned)((t + 1) & 3) * S2_STG;
        s2_half<NN, true, true, 0>(acc, a, b, lo, hi, a_ad + sn, bn_ad + sn, bx_ad + sn, by_ad + sn);
    }
    LGKM0(); SB0();
}

#define S2_NL 8
constexpr int S2_G = (34 + S2_NL - 1) / S2_NL;

__global__ __launch_bounds__((8 + S2_NL) * 64, (8 + S2_NL) / 4) void stage2_kernel(const bf16_t* __restrict__ cre, const bf16_t* __restrict__ cim,
                                                        const bf16_t* __restrict__ abT, const bf16_t* __restrict__ hbuf,
                                                        const bf16_t* __restrict__ wd, float* pbuf, unsigned* cnt,
                                                        const bf16_t* __restrict__ x, bf16_t* __restrict__ out) {
    constexpr int STG = S2_STG;
    __shared__ __attribute__((aligned(1024))) char shm[4 * STG];
    const int tid = threadIdx.x, widx = __builtin_amdgcn_readfirstlane(tid >> 6), lane = tid & 63;
    const int xcd = blockIdx.x & 7, idx = blockIdx.x >> 3;
    const int ty = xcd & 3, m0 = ((xcd >> 2) * 8 + (idx >> 2)) * 128, n0 = (idx & 3) * 144;
    const int fr = lane & 15, fq = lane >> 4, xr7 = lane & 7;
    const bool nn = ty >= 2;
    const int nkt = nn ? 36 : 32;
    if (widx >= 8) {
        const int lw = widx - 8;
        const int srow = lane >> 3, schunk = (lane & 7) ^ srow;
        const int lda = nn ? F_ : S_;
        const bf16_t* abase = ty == 0 ? cre : (ty == 1 ? cim : (ty == 2 ? hbuf : hbuf + 2304));
        const bf16_t* src[S2_G]; size_t step[S2_G]; int dst[S2_G];
#pragma unroll
        for (int j = 0; j < S2_G; ++j) {
            int i = lw + S2_NL * j; if (i > 33) i = 33;
            dst[j] = i * 1024;
            if (i < 16) { src[j] = abase + (size_t)(m0 + i * 8 + srow) * lda + schunk * 8; step[j] = 64; }
            else if (!nn) { src[j] = abT + (size_t)(ty * D_ + n0 + (i - 16) * 8 + srow) * S_ + schunk * 8; step[j] = 64; }
            else { src[j] = wd + (size_t)(n0 + (i - 16) * 8 + srow) * F_ + (ty - 2) * 2304 + schunk * 8; step[j] = 64; }
        }
        auto stage = [&](int buf, int kt) {
            char* base = shm + buf * STG;
#pragma unroll
            for (int j = 0; j < S2_G; ++j) glds16(src[j] + (size_t)kt * step[j], base + dst[j]);
        };
        stage(0, 0); stage(1, 1); stage(2, 2);
        wait_vm<2 * S2_G>(); __builtin_amdgcn_s_barrier(); asm volatile("" ::: "memory");
#pragma unroll 1
        for (int t = 0; t < nkt; ++t) {
            if (t + 3 < nkt) { stage((t + 3) & 3, t + 3); wait_vm<2 * S2_G>(); }
            else if (t + 2 < nkt) wait_vm<S2_G>();
            else wait_vm<0>();
            __builtin_amdgcn_s_barrier(); asm volatile("" ::: "memory");
        }
        return;
    }
    const int wid = widx & 3, kh = widx >> 2;
    const int g = lane >> 4, q = (lane & 15) >> 2, p = lane & 3;
    const unsigned lbase = (unsigned)(size_t)shm;
    const unsigned co0 = (unsigned)(((fq ^ xr7) * 16) ^ (kh * 64));
    const unsigned a_ad = lbase + (wid * 32 + fr) * 128 + co0;
    const unsigned bn_ad = lbase + fr * 128 + co0;
    const unsigned rb = lbase + (8 * g + q) * 288 + p * 8 + kh * 9216;
    const unsigned bx_ad = rb + (g & 1) * 128, by_ad = rb - (g & 1) * 160;
    f32x4 acc[9][2] = {};
    s2_compute<false>(acc, nkt, a_ad, bn_ad, bx_ad, by_ad);
    __builtin_amdgcn_s_barrier(); asm volatile("" ::: "memory");
    f32x4* red = (f32x4*)shm + wid * 18 * 64 + lane;
    if (kh == 1) {
#pragma unroll
        for (int i = 0; i < 18; ++i) red[i * 64] = acc[i >> 1][i & 1];
    }
    __syncthreads();
    int lz = lane; asm volatile("" : "+v"(lz));
    const __amdgpu_buffer_rsrc_t prs = __builtin_amdgcn_make_buffer_rsrc(pbuf, 0, 4u * S_ * D_ * 4u, 0x00020000);
    if (kh == 0) {
        const int fr2 = lz & 15, fq2 = lz >> 4;
#pragma unroll
        for (int mt = 0; mt < 2; ++mt)
#pragma unroll
            for (int n = 0; n < 9; ++n) {
                const int tok = m0 + wid * 32 + mt * 16 + fr2, d = n0 + n * 16 + fq2 * 4;
                const f32x4 v = acc[n][mt] + red[(n * 2 + mt) * 64];
                __builtin_amdgcn_raw_buffer_store_b128(__builtin_bit_cast(u32x4, v), prs, (unsigned)(((ty * S_ + tok) * D_ + d) * 4), 0, 16);
            }
        asm volatile("s_waitcnt vmcnt(0)" ::: "memory");
    }
    __syncthreads();
    const int tile = (m0 >> 7) * 4 + (idx & 3);
    if (tid == 0) {
        __hip_atomic_fetch_add(cnt + tile * 32, 1u, __ATOMIC_RELAXED, __HIP_MEMORY_SCOPE_AGENT);
        for (unsigned spins = 0; spins < (1u << 22); ++spins) {
            if (__hip_atomic_load(cnt + tile * 32, __ATOMIC_RELAXED, __HIP_MEMORY_SCOPE_AGENT) >= 4u) break;
            __builtin_amdgcn_s_sleep(2);
        }
    }
    __syncthreads();
    for (int i = tid; i < 32 * 36; i += 512) {
        const int tok = m0 + ty * 32 + i / 36, d = n0 + (i % 36) * 4;
        const unsigned o = (unsigned)((tok * D_ + d) * 4);
        f32x4 pv[4];
#pragma unroll
        for (int k = 0; k < 4; ++k) pv[k] = __builtin_bit_cast(f32x4, __builtin_amdgcn_raw_buffer_load_b128(prs, o + (unsigned)k * (S_ * D_ * 4u), 0, 16));
        const u32x2 xv = *(const u32x2*)(x + (size_t)tok * D_ + d);
        float r[4];
#pragma unroll
        for (int j = 0; j < 4; ++j) {
            const float sv = rbf(rbf(pv[0][j]) - rbf(pv[1][j]));
            const float dv = rbf(pv[2][j] + pv[3][j]);
            const float sl = rbf(dv * __builtin_amdgcn_rcpf(1.0f + __expf(-dv)));
            const float xf = (j & 1) ? hi_f(xv[j >> 1]) : lo_f(xv[j >> 1]);
            r[j] = xf + rbf(sv * sl);
        }
        u32x2 ov; ov[0] = cvt_pk(r[0], r[1]); ov[1] = cvt_pk(r[2], r[3]);
        *(u32x2*)(out + (size_t)tok * D_ + d) = ov;
    }
}

extern "C" void kernel_launch(void* const* d_in, const int* in_sizes, int n_in,
                              void* d_out, int out_size, void* d_ws, size_t ws_size,
                              hipStream_t stream) {
    const bf16_t* x = (const bf16_t*)d_in[0];
    const bf16_t* cre = (const bf16_t*)d_in[1];
    const bf16_t* cim = (const bf16_t*)d_in[2];
    const bf16_t* wre = (const bf16_t*)d_in[3];
    const bf16_t* wim = (const bf16_t*)d_in[4];
    const bf16_t* wg = (const bf16_t*)d_in[6];
    const bf16_t* wu = (const bf16_t*)d_in[7];
    const bf16_t* wd = (const bf16_t*)d_in[8];
    bf16_t* out = (bf16_t*)d_out;
    char* ws = (char*)d_ws;
    bf16_t* xn = (bf16_t*)ws;                       ws += (size_t)S_ * D_ * 2;
    bf16_t* abT = (bf16_t*)ws;                      ws += (size_t)2 * D_ * S_ * 2;
    bf16_t* hbuf = (bf16_t*)ws;                     ws += (size_t)S_ * F_ * 2;
    float* pbuf = (float*)ws;                       ws += (size_t)4 * S_ * D_ * 4;
    bf16_t* wdT = (bf16_t*)ws;                      ws += (size_t)D_ * F_ * 2;
    unsigned* cnt = (unsigned*)ws;                  ws += 64 * 32 * 4;
    ln_kernel<<<PREP_LN + PREP_TR + 2 * PREP_PFW + 2 * PREP_PFH, 256, 0, stream>>>(x, xn, wg, wu, wre, wim, wd, wdT, cnt);
    stage1_kernel<<<256, 512, 0, stream>>>(xn, wg, wu, wre, wim, hbuf, abT, cre, cim);
    stage2_kernel<<<256, (8 + S2_NL) * 64, 0, stream>>>(cre, cim, abT, hbuf, wdT, pbuf, cnt, x, out);
}
```

```cpp
#include <hip/hip_runtime.h>
#include <hip/hip_bf16.h>

typedef unsigned short bf16_t;
typedef short bf16x8 __attribute__((ext_vector_type(8)));
typedef short s16x4 __attribute__((ext_vector_type(4)));
typedef float f32x4 __attribute__((ext_vector_type(4)));
typedef unsigned u32x2 __attribute__((ext_vector_type(2)));
typedef unsigned u32x4 __attribute__((ext_vector_type(4)));

#define LAS __attribute__((address_space(3)))

constexpr int S_ = 2048, D_ = 576, F_ = 4608;

__device__ __forceinline__ float bf2f(unsigned short u) { return __uint_as_float(((unsigned)u) << 16); }
__device__ __forceinline__ unsigned short f2bf(float f) { return __builtin_bit_cast(unsigned short, (__bf16)f); }
__device__ __forceinline__ float rbf(float f) { return bf2f(f2bf(f)); }

__device__ __forceinline__ void glds16(const void* g, char* l) {
    __builtin_amdgcn_global_load_lds((const unsigned*)g, (LAS unsigned*)l, 16, 0, 0);
}

constexpr int PREP_LN = S_ / 4, PREP_TR = (F_ / 64) * (D_ / 64), PREP_PFW = D_ * F_ * 2 / 32768, PREP_PFH = (D_ * D_ * 2 + 32767) / 32768;
__global__ __launch_bounds__(256) void ln_kernel(const bf16_t* __restrict__ x, bf16_t* __restrict__ xn,
                                                 const bf16_t* __restrict__ p0, const bf16_t* __restrict__ p1,
                                                 const bf16_t* __restrict__ p2, const bf16_t* __restrict__ p3,
                                                 const bf16_t* __restrict__ wd, bf16_t* __restrict__ wdT, unsigned* __restrict__ cnt) {
    __shared__ unsigned short tile[64][66];
    if (blockIdx.x == 0) { for (int i = threadIdx.x; i < 64 * 32; i += 256) cnt[i] = 0u; }
    if (blockIdx.x >= PREP_LN + PREP_TR) {
        int bb = blockIdx.x - PREP_LN - PREP_TR;
        const char* base; size_t lim;
        if (bb < PREP_PFW) { base = (const char*)p0; lim = (size_t)D_ * F_ * 2; }
        else if ((bb -= PREP_PFW) < PREP_PFW) { base = (const char*)p1; lim = (size_t)D_ * F_ * 2; }
        else if ((bb -= PREP_PFW) < PREP_PFH) { base = (const char*)p2; lim = (size_t)D_ * D_ * 2; }
        else { bb -= PREP_PFH; base = (const char*)p3; lim = (size_t)D_ * D_ * 2; }
        u32x4 v[8];
#pragma unroll
        for (int i = 0; i < 8; ++i) {
            size_t o = (size_t)bb * 32768 + i * 4096 + threadIdx.x * 16;
            if (o >= lim) o = 0;
            v[i] = *(const u32x4*)(base + o);
        }
#pragma unroll
        for (int i = 0; i < 8; ++i) asm volatile("" ::"v"(v[i]));
        return;
    }
    if (blockIdx.x >= PREP_LN) {
        const int b = blockIdx.x - PREP_LN, f0 = (b / 9) * 64, d0 = (b % 9) * 64;
        const int r = threadIdx.x >> 3, c = (threadIdx.x & 7) * 8;
#pragma unroll
        for (int h = 0; h < 2; ++h) {
            const bf16x8 v = *(const bf16x8*)(wd + (size_t)(f0 + r + 32 * h) * D_ + d0 + c);
#pragma unroll
            for (int j = 0; j < 8; ++j) tile[r + 32 * h][c + j] = (unsigned short)v[j];
        }
        __syncthreads();
#pragma unroll
        for (int h = 0; h < 2; ++h) {
            bf16x8 v;
#pragma unroll
            for (int j = 0; j < 8; ++j) v[j] = (short)tile[c + j][r + 32 * h];
            *(bf16x8*)(wdT + (size_t)(d0 + r + 32 * h) * F_ + f0 + c) = v;
        }
        return;
    }
    const int row = blockIdx.x * 4 + (threadIdx.x >> 6), lane = threadIdx.x & 63;
    const bf16_t* xr = x + (size_t)row * D_;
    bf16x8 v0 = *(const bf16x8*)(xr + lane * 8);
    bf16x8 v1 = {0, 0, 0, 0, 0, 0, 0, 0};
    const bool has2 = lane < 8;
    if (has2) v1 = *(const bf16x8*)(xr + 512 + lane * 8);
    float f[16];
    float s = 0.f;
#pragma unroll
    for (int j = 0; j < 8; ++j) { f[j] = bf2f((unsigned short)v0[j]); f[8 + j] = bf2f((unsigned short)v1[j]); s += f[j] + f[8 + j]; }
#pragma unroll
    for (int o = 32; o > 0; o >>= 1) s += __shfl_xor(s, o);
    const float mean = s * (1.0f / D_);
    float q = 0.f;
#pragma unroll
    for (int j = 0; j < 8; ++j) { float d0 = f[j] - mean; q += d0 * d0; if (has2) { float d1 = f[8 + j] - mean; q += d1 * d1; } }
#pragma unroll
    for (int o = 32; o > 0; o >>= 1) q += __shfl_xor(q, o);
    const float var = q * (1.0f / D_);
    const float mb = rbf(mean), vb = rbf(var);
    const float den = rbf(sqrtf(rbf(vb + 1e-5f)));
    bf16x8 o0, o1;
#pragma unroll
    for (int j = 0; j < 8; ++j) {
        o0[j] = (short)f2bf(rbf(f[j] - mb) / den);
        o1[j] = (short)f2bf(rbf(f[8 + j] - mb) / den);
    }
    bf16_t* orow = xn + (size_t)row * D_;
    *(bf16x8*)(orow + lane * 8) = o0;
    if (has2) *(bf16x8*)(orow + 512 + lane * 8) = o1;
}

#define LGKM0() asm volatile("s_waitcnt lgkmcnt(0)" ::: "memory")
#define SB0() __builtin_amdgcn_sched_barrier(0)
#define DSR128(dst, addr, off) asm volatile("ds_read_b128 %0, %1 offset:%2" : "=v"(dst) : "v"(addr), "i"(off))
#define DSR64(dst, addr, off) asm volatile("ds_read_b64 %0, %1 offset:%2" : "=v"(dst) : "v"(addr), "i"(off))
#define DSRTR(dst, addr, off) asm volatile("ds_read_b64_tr_b16 %0, %1 offset:%2" : "=v"(dst) : "v"(addr), "i"(off))
template <int N> __device__ __forceinline__ void wait_lgkm() { asm volatile("s_waitcnt lgkmcnt(%0)" ::"n"(N > 15 ? 15 : N) : "memory"); }
template <int N> __device__ __forceinline__ void wait_vm() { asm volatile("s_waitcnt vmcnt(%0)" ::"n"(N) : "memory"); }

constexpr int S1_A = 0, S1_G = 32768, S1_U = 51200, S1_E = 69632, S1_STG = 75776;

typedef float f32x2 __attribute__((ext_vector_type(2)));
typedef __bf16 bf16x2v __attribute__((ext_vector_type(2)));
__device__ __forceinline__ unsigned cvt_pk(float lo, float hi) { f32x2 v = {lo, hi}; return __builtin_bit_cast(unsigned, __builtin_convertvector(v, bf16x2v)); }
__device__ __forceinline__ float lo_f(unsigned p) { return __uint_as_float(p << 16); }
__device__ __forceinline__ float hi_f(unsigned p) { return __uint_as_float(p & 0xffff0000u); }
__device__ __forceinline__ unsigned glu_pair(float g0, float g1, float u0, float u1) {
    const unsigned gp = cvt_pk(g0, g1), up = cvt_pk(u0, u1);
    const float e0 = __builtin_amdgcn_exp2f(lo_f(gp) * -1.4426950408889634f), e1 = __builtin_amdgcn_exp2f(hi_f(gp) * -1.4426950408889634f);
    const unsigned sp = cvt_pk(__builtin_amdgcn_rcpf(1.0f + e0), __builtin_amdgcn_rcpf(1.0f + e1));
    return cvt_pk(lo_f(sp) * lo_f(up), hi_f(sp) * hi_f(up));
}

template <int NF, int NE> struct S1Sched {
    static constexpr int NG = 2 * NF + NE;
    static constexpr int ninst(int i) { return 2; }
    static constexpr int younger(int i) {
        int n = 0;
        for (int g = (i == 0 ? 0 : i - 1); g <= i; ++g) {
            if (i >= 2 && g == i - 2) continue;
            if (g == 2) n += 4;
            if (g + 2 < 2 * NG) n += ninst(g + 2);
        }
        if (i == 0) n += ninst(1);
        return n;
    }
};

template <int NF, int NE, int F0T, int E0T, bool HAS_E>
__device__ __forceinline__ void s1_body(char* shm, int lane, int wm,
                                        const bf16_t* xn, const bf16_t* wg, const bf16_t* wu, const bf16_t* we,
                                        int wid, int m0, int f0, int e0, bf16_t* hbuf, bf16_t* abT, const char* pf) {
    constexpr int NT = D_ / 64;
    asm volatile("" : "+v"(lane));
    const int srow = lane >> 3, schunk = (lane & 7) ^ srow;
    const unsigned a_off = (unsigned)(((m0 + wid * 32 + srow) * D_ + schunk * 8) * 2);
    unsigned b_off[5];
#pragma unroll
    for (int j = 0; j < 5; ++j) {
        int i = wid + 8 * j; if (i > 35) i = 35;
        const int ii = i < 18 ? i : i - 18;
        const int P = ii * 64 + lane, k = P / 18, c16p = P % 18, c32p = c16p >> 1;
        const int c32 = (c32p + 9 - 4 * ((k >> 3) & 1)) % 9;
        b_off[j] = (unsigned)((k * F_ + f0 + (2 * c32 + (c16p & 1)) * 8) * 2);
    }
    const unsigned e_off = (unsigned)((((e0 < D_ ? e0 : e0 - D_) + (wid < 6 ? wid : 5) * 8 + srow) * D_ + schunk * 8) * 2);
    constexpr int NEE = HAS_E ? NE : 0;
    using SC = S1Sched<NF, NEE>;
    constexpr int NG = SC::NG;
    const int fr = lane & 15, fq = lane >> 4, xr7 = lane & 7;
    const int g = lane >> 4, q = (lane & 15) >> 2, p = lane & 3;
    const unsigned lbase = (unsigned)(size_t)shm;
    const unsigned co0 = (unsigned)((fq ^ xr7) * 16);
    const unsigned a_ad = lbase + S1_A + (wm * 64 + fr) * 128 + co0;
    const unsigned e_ad = lbase + S1_E + fr * 128 + co0;
    const unsigned rb = lbase + S1_G + (8 * g + q) * 288 + p * 8;
    const unsigned bx_ad = rb + (g & 1) * 128, by_ad = rb - (g & 1) * 160;
    auto gl = [&](int j, int buf, int kt) {
        char* base = shm + buf * S1_STG;
        if (j < 4) glds16((const char*)(xn + j * 8 * D_ + kt * 64) + a_off, base + S1_A + (wid * 4 + j) * 1024);
        else if (j < 9) { int i = wid + 8 * (j - 4); if (i > 35) i = 35;
            glds16((const char*)((i < 18 ? wg : wu) + kt * 64 * F_) + b_off[j - 4], base + S1_G + i * 1024); }
        else if (HAS_E) glds16((const char*)(we + kt * 64) + e_off, base + S1_E + (wid < 6 ? wid : 5) * 1024);
    };
    f32x4 accg[NF][4] = {}, accu[NF][4] = {}, acce[NE][4] = {};
    bf16x8 af[2][4];
    s16x4 lo[3], hi[3];
#pragma unroll
    for (int j = 0; j < 10; ++j) gl(j, 0, 0);
#define S1_RD(i, sl) do { constexpr int gi_ = (i) % NG, ks_ = (i) / NG, w_ = (i) % 3; \
        if constexpr (gi_ < 2 * NF) { constexpr int c_ = F0T + gi_ / 2, off_ = (gi_ & 1) * 18432 + ks_ * 9216 + c_ * 32; \
            if constexpr (c_ < 5) { DSRTR(lo[w_], bx_ad + (sl), off_); DSRTR(hi[w_], bx_ad + (sl), off_ + 1152); } \
            else { DSRTR(lo[w_], by_ad + (sl), off_); DSRTR(hi[w_], by_ad + (sl), off_ + 1152); } } \
        else { DSR64(lo[w_], (e_ad + (sl)) ^ (ks_ * 64), (E0T + gi_ - 2 * NF) * 2048); DSR64(hi[w_], (e_ad + (sl)) ^ (ks_ * 64), (E0T + gi_ - 2 * NF) * 2048 + 8); } } while (0)
#pragma unroll 1
    for (int t = 0; t < NT; ++t) {
        const unsigned sl = (unsigned)(t & 1) * S1_STG;
        if (t == 0) wait_vm<0>(); else wait_vm<1>();
        __builtin_amdgcn_s_barrier(); asm volatile("" ::: "memory");
        const bool more = t + 1 < NT;
#pragma unroll
        for (int mt = 0; mt < 4; ++mt) DSR128(af[0][mt], a_ad + sl, mt * 2048);
        S1_RD(0, sl); S1_RD(1, sl);
        SB0();
#define S1_GROUP(i) do { constexpr int gi = (i) % NG, ks = (i) / NG, w = (i) % 3; \
            if constexpr ((i) == 2) { _Pragma("unroll") for (int mt = 0; mt < 4; ++mt) DSR128(af[1][mt], (a_ad + sl) ^ 64, mt * 2048); } \
            if constexpr ((i) + 2 < 2 * NG) S1_RD((i) + 2, sl); \
            if constexpr ((i) % 2 == 0 && (i) / 2 < 10) { if (more) gl((i) / 2, (t + 1) & 1, t + 1); } \
            if constexpr ((i) == 2 * NG - 2) { if (more) glds16(pf + (wid * 8 + t) * 1024 + lane * 16, shm + 2 * S1_STG + wid * 1024); } \
            wait_lgkm<SC::younger(i)>(); SB0(); \
            if constexpr (gi < 2 * NF) { const bf16x8 bf = __builtin_shufflevector(lo[w], hi[w], 0, 1, 2, 3, 4, 5, 6, 7); \
                _Pragma("unroll") for (int mt = 0; mt < 4; ++mt) { \
                    if constexpr ((gi & 1) == 0) accg[gi / 2][mt] = __builtin_amdgcn_mfma_f32_16x16x32_bf16(bf, af[ks][mt], accg[gi / 2][mt], 0, 0, 0); \
                    else                         accu[gi / 2][mt] = __builtin_amdgcn_mfma_f32_16x16x32_bf16(bf, af[ks][mt], accu[gi / 2][mt], 0, 0, 0); } } \
            else { const bf16x8 bf = __builtin_shufflevector(lo[w], hi[w], 0, 1, 2, 3, 4, 5, 6, 7); \
                _Pragma("unroll") for (int mt = 0; mt < 4; ++mt) \
                    acce[gi - 2 * NF][mt] = __builtin_amdgcn_mfma_f32_16x16x32_bf16(af[ks][mt], bf, acce[gi - 2 * NF][mt], 0, 0, 0); } \
            SB0(); } while (0)
        S1_GROUP(0); S1_GROUP(1); S1_GROUP(2); S1_GROUP(3); S1_GROUP(4); S1_GROUP(5); S1_GROUP(6); S1_GROUP(7);
        S1_GROUP(8); S1_GROUP(9); S1_GROUP(10); S1_GROUP(11); S1_GROUP(12); S1_GROUP(13); S1_GROUP(14); S1_GROUP(15);
        if constexpr (2 * NG > 16) { S1_GROUP(16); S1_GROUP(17); }
        if constexpr (2 * NG > 18) { S1_GROUP(18); S1_GROUP(19); }
        if constexpr (2 * NG > 20) { S1_GROUP(20); S1_GROUP(21); }
    }
    constexpr int HROW = 304, EROW = 528, EBASE = 256 * HROW;
    __builtin_amdgcn_s_barrier(); asm volatile("" ::: "memory");
    int lz = lane; asm volatile("" : "+v"(lz));
    const int fr2 = lz & 15, fq2 = lz >> 4;
#pragma unroll
    for (int mt = 0; mt < 4; ++mt)
#pragma unroll
        for (int n = 0; n < NF; ++n) {
            u32x2 o;
            o[0] = glu_pair(accg[n][mt][0], accg[n][mt][1], accu[n][mt][0], accu[n][mt][1]);
            o[1] = glu_pair(accg[n][mt][2], accg[n][mt][3], accu[n][mt][2], accu[n][mt][3]);
            *(u32x2*)(shm + (wm * 64 + mt * 16 + fr2) * HROW + ((F0T + n) * 16 + fq2 * 4) * 2) = o;
        }
    if constexpr (HAS_E) {
#pragma unroll
        for (int mt = 0; mt < 4; ++mt)
#pragma unroll
            for (int n = 0; n < NE; ++n) {
                u32x2 o; o[0] = cvt_pk(acce[n][mt][0], acce[n][mt][1]); o[1] = cvt_pk(acce[n][mt][2], acce[n][mt][3]);
                *(u32x2*)(shm + EBASE + ((E0T + n) * 16 + fr2) * EROW + (wm * 64 + mt * 16 + fq2 * 4) * 2) = o;
            }
    }
    asm volatile("s_waitcnt lgkmcnt(0)" ::: "memory");
    __builtin_amdgcn_s_barrier(); asm volatile("" ::: "memory");
    const int tz = wid * 64 + lz;
#pragma unroll
    for (int i = 0; i < 9; ++i) {
        const int c = tz + i * 512, row = c / 18, ch = c % 18;
        const u32x4 v = *(const u32x4*)(shm + row * HROW + ch * 16);
        *(u32x4*)(hbuf + (size_t)(m0 + row) * F_ + f0 + ch * 8) = v;
    }
    if constexpr (HAS_E) {
#pragma unroll
        for (int i = 0; i < 3; ++i) {
            const int c = tz + i * 512, row = c >> 5, ch = c & 31;
            const u32x4 v = *(const u32x4*)(shm + EBASE + row * EROW + ch * 16);
            *(u32x4*)(abT + (size_t)(e0 + row) * S_ + m0 + ch * 8) = v;
        }
    }
    wait_vm<0>();
}

__global__ __launch_bounds__(512, 2) void stage1_kernel(const bf16_t* __restrict__ xn, const bf16_t* __restrict__ wg,
                                                        const bf16_t* __restrict__ wu, const bf16_t* __restrict__ wre,
                                                        const bf16_t* __restrict__ wim, bf16_t* __restrict__ hbuf,
                                                        bf16_t* __restrict__ abT, const bf16_t* __restrict__ cre,
                                                        const bf16_t* __restrict__ cim) {
    __shared__ __attribute__((aligned(1024))) char shm[2 * S1_STG + 8192];
    const int tid = threadIdx.x, wid = __builtin_amdgcn_readfirstlane(tid >> 6), lane = tid & 63;
    const int xcd = blockIdx.x & 7, idx = blockIdx.x >> 3;
    const int fg = xcd * 4 + (idx >> 3), m0 = (idx & 7) * 256, f0 = fg * 144;
    const bool has_dft = (fg & 3) != 3;
    const int e0 = ((fg >> 2) * 3 + (fg & 3)) * 48;
    const bf16_t* we = e0 < D_ ? wre : wim;
    const char* pf = (const char*)(blockIdx.x < 128 ? cre : cim) + (size_t)(blockIdx.x & 127) * 65536;
    const int wm = wid & 3;
    if (has_dft) {
        if (wid < 4) s1_body<5, 1, 0, 0, true>(shm, lane, wm, xn, wg, wu, we, wid, m0, f0, e0, hbuf, abT, pf);
        else         s1_body<4, 2, 5, 1, true>(shm, lane, wm, xn, wg, wu, we, wid, m0, f0, e0, hbuf, abT, pf);
    } else {
        if (wid < 4) s1_body<5, 1, 0, 0, false>(shm, lane, wm, xn, wg, wu, we, wid, m0, f0, e0, hbuf, abT, pf);
        else         s1_body<4, 2, 5, 1, false>(shm, lane, wm, xn, wg, wu, we, wid, m0, f0, e0, hbuf, abT, pf);
    }
}

constexpr int S2_STG = 16384 + 18432;

template <bool NN, bool DO_READ, bool BAR, int KSN>
__device__ __forceinline__ void s2_half(f32x4 (&acc)[9][2], bf16x8 (&a)[2], bf16x8 (&b)[9], s16x4 (&lo)[9], s16x4 (&hi)[9],
                                        unsigned aa, unsigned bb, unsigned bx, unsigned by) {
#pragma unroll
    for (int j = 0; j < 18; ++j) {
        const int n = j % 9, mt = j / 9;
        if (j < 9) { if constexpr (NN) { if (j == 0) wait_lgkm<17>(); else if (j == 1) wait_lgkm<15>(); else if (j == 2) wait_lgkm<13>(); else if (j == 3) wait_lgkm<11>();
                                         else if (j == 4) wait_lgkm<9>(); else if (j == 5) wait_lgkm<7>(); else if (j == 6) wait_lgkm<5>(); else if (j == 7) wait_lgkm<3>(); else wait_lgkm<1>(); }
                     else { if (j == 0) wait_lgkm<9>(); else if (j == 1) wait_lgkm<8>(); else if (j == 2) wait_lgkm<7>(); else if (j == 3) wait_lgkm<6>();
                            else if (j == 4) wait_lgkm<5>(); else if (j == 5) wait_lgkm<4>(); else if (j == 6) wait_lgkm<3>(); else if (j == 7) wait_lgkm<2>(); else wait_lgkm<1>(); } }
        else if (j == 9) wait_lgkm<0>();
        SB0();
        bf16x8 bf;
        if constexpr (NN) bf = __builtin_shufflevector(lo[n], hi[n], 0, 1, 2, 3, 4, 5, 6, 7); else bf = b[n];
        acc[n][mt] = __builtin_amdgcn_mfma_f32_16x16x32_bf16(bf, a[mt], acc[n][mt], 0, 0, 0);
        SB0();
        if (j == 9 && BAR) { __builtin_amdgcn_s_barrier(); asm volatile("" ::: "memory"); }
        if constexpr (DO_READ) {
            if (j == 9) DSR128(a[0], aa, 0);
            if (j >= 9) {
                if constexpr (!NN) DSR128(b[n], bb, 16384 + n * 2048);
                else {
                    if (n < 5) { DSRTR(lo[n], bx, 16384 + KSN * 9216 + n * 32); DSRTR(hi[n], bx, 16384 + KSN * 9216 + n * 32 + 1152); }
                    else       { DSRTR(lo[n], by, 16384 + KSN * 9216 + n * 32); DSRTR(hi[n], by, 16384 + KSN * 9216 + n * 32 + 1152); }
                }
            }
            if (j == 17) DSR128(a[1], aa, 2048);
        }
        SB0();
    }
}
template <bool NN>
__device__ __forceinline__ void s2_compute(f32x4 (&acc)[9][2], int nkt, unsigned a_ad, unsigned bn_ad, unsigned bx_ad, unsigned by_ad) {
    bf16x8 a[2], b[9]; s16x4 lo[9], hi[9];
    __builtin_amdgcn_s_barrier(); asm volatile("" ::: "memory");
    DSR128(a[0], a_ad, 0);
#pragma unroll
    for (int n = 0; n < 9; ++n) {
        if constexpr (!NN) DSR128(b[n], bn_ad, 16384 + n * 2048);
        else {
            if (n < 5) { DSRTR(lo[n], bx_ad, 16384 + n * 32); DSRTR(hi[n], bx_ad, 16384 + n * 32 + 1152); }
            else       { DSRTR(lo[n], by_ad, 16384 + n * 32); DSRTR(hi[n], by_ad, 16384 + n * 32 + 1152); }
        }
    }
    DSR128(a[1], a_ad, 2048);
    SB0();
#pragma unroll 1
    for (int t = 0; t < nkt; ++t) {
        const unsigned sn = (unsigned)((t + 1) & 3) * S2_STG;
        s2_half<NN, true, true, 0>(acc, a, b, lo, hi, a_ad + sn, bn_ad + sn, bx_ad + sn, by_ad + sn);
    }
    LGKM0(); SB0();
}

#define S2_NL 8
constexpr int S2_G = (34 + S2_NL - 1) / S2_NL;

__global__ __launch_bounds__((8 + S2_NL) * 64, (8 + S2_NL) / 4) void stage2_kernel(const bf16_t* __restrict__ cre, const bf16_t* __restrict__ cim,
                                                        const bf16_t* __restrict__ abT, const bf16_t* __restrict__ hbuf,
                                                        const bf16_t* __restrict__ wd, float* pbuf, unsigned* cnt,
                                                        const bf16_t* __restrict__ x, bf16_t* __restrict__ out) {
    constexpr int STG = S2_STG;
    __shared__ __attribute__((aligned(1024))) char shm[4 * STG];
    const int tid = threadIdx.x, widx = __builtin_amdgcn_readfirstlane(tid >> 6), lane = tid & 63;
    const int xcd = blockIdx.x & 7, idx = blockIdx.x >> 3;
    const int ty = xcd & 3, m0 = ((xcd >> 2) * 8 + (idx >> 2)) * 128, n0 = (idx & 3) * 144;
    const int fr = lane & 15, fq = lane >> 4, xr7 = lane & 7;
    const bool nn = ty >= 2;
    const int nkt = nn ? 36 : 32;
    if (widx >= 8) {
        const int lw = widx - 8;
        const int srow = lane >> 3, schunk = (lane & 7) ^ srow;
        const int lda = nn ? F_ : S_;
        const bf16_t* abase = ty == 0 ? cre : (ty == 1 ? cim : (ty == 2 ? hbuf : hbuf + 2304));
        const bf16_t* src[S2_G]; size_t step[S2_G]; int dst[S2_G];
#pragma unroll
        for (int j = 0; j < S2_G; ++j) {
            int i = lw + S2_NL * j; if (i > 33) i = 33;
            dst[j] = i * 1024;
            if (i < 16) { src[j] = abase + (size_t)(m0 + i * 8 + srow) * lda + schunk * 8; step[j] = 64; }
            else if (!nn) { src[j] = abT + (size_t)(ty * D_ + n0 + (i - 16) * 8 + srow) * S_ + schunk * 8; step[j] = 64; }
            else { src[j] = wd + (size_t)(n0 + (i - 16) * 8 + srow) * F_ + (ty - 2) * 2304 + schunk * 8; step[j] = 64; }
        }
        auto stage = [&](int buf, int kt) {
            char* base = shm + buf * STG;
#pragma unroll
            for (int j = 0; j < S2_G; ++j) glds16(src[j] + (size_t)kt * step[j], base + dst[j]);
        };
        stage(0, 0); stage(1, 1); stage(2, 2);
        wait_vm<2 * S2_G>(); __builtin_amdgcn_s_barrier(); asm volatile("" ::: "memory");
#pragma unroll 1
        for (int t = 0; t < nkt; ++t) {
            if (t + 3 < nkt) { stage((t + 3) & 3, t + 3); wait_vm<2 * S2_G>(); }
            else if (t + 2 < nkt) wait_vm<S2_G>();
            else wait_vm<0>();
            __builtin_amdgcn_s_barrier(); asm volatile("" ::: "memory");
        }
        return;
    }
    const int wid = widx & 3, kh = widx >> 2;
    const int g = lane >> 4, q = (lane & 15) >> 2, p = lane & 3;
    const unsigned lbase = (unsigned)(size_t)shm;
    const unsigned co0 = (unsigned)(((fq ^ xr7) * 16) ^ (kh * 64));
    const unsigned a_ad = lbase + (wid * 32 + fr) * 128 + co0;
    const unsigned bn_ad = lbase + fr * 128 + co0;
    const unsigned rb = lbase + (8 * g + q) * 288 + p * 8 + kh * 9216;
    const unsigned bx_ad = rb + (g & 1) * 128, by_ad = rb - (g & 1) * 160;
    f32x4 acc[9][2] = {};
    s2_compute<false>(acc, nkt, a_ad, bn_ad, bx_ad, by_ad);
    __builtin_amdgcn_s_barrier(); asm volatile("" ::: "memory");
    f32x4* red = (f32x4*)shm + wid * 18 * 64 + lane;
    if (kh == 1) {
#pragma unroll
        for (int i = 0; i < 18; ++i) red[i * 64] = acc[i >> 1][i & 1];
    }
    __syncthreads();
    int lz = lane; asm volatile("" : "+v"(lz));
    const __amdgpu_buffer_rsrc_t prs = __builtin_amdgcn_make_buffer_rsrc(pbuf, 0, 4u * S_ * D_ * 4u, 0x00020000);
    if (kh == 0) {
        const int fr2 = lz & 15, fq2 = lz >> 4;
#pragma unroll
        for (int mt = 0; mt < 2; ++mt)
#pragma unroll
            for (int n = 0; n < 9; ++n) {
                const int tok = m0 + wid * 32 + mt * 16 + fr2, d = n0 + n * 16 + fq2 * 4;
                const f32x4 v = acc[n][mt] + red[(n * 2 + mt) * 64];
                __builtin_amdgcn_raw_buffer_store_b128(__builtin_bit_cast(u32x4, v), prs, (unsigned)(((ty * S_ + tok) * D_ + d) * 4), 0, 16);
            }
        asm volatile("s_waitcnt vmcnt(0)" ::: "memory");
    }
    __syncthreads();
    const int tile = (m0 >> 7) * 4 + (idx & 3);
    if (tid == 0) {
        __hip_atomic_fetch_add(cnt + tile * 32, 1u, __ATOMIC_RELAXED, __HIP_MEMORY_SCOPE_AGENT);
        for (unsigned spins = 0; spins < (1u << 22); ++spins) {
            if (__hip_atomic_load(cnt + tile * 32, __ATOMIC_RELAXED, __HIP_MEMORY_SCOPE_AGENT) >= 4u) break;
            __builtin_amdgcn_s_sleep(2);
        }
    }
    __syncthreads();
    for (int i = tid; i < 32 * 36; i += 512) {
        const int tok = m0 + ty * 32 + i / 36, d = n0 + (i % 36) * 4;
        const unsigned o = (unsigned)((tok * D_ + d) * 4);
        f32x4 pv[4];
#pragma unroll
        for (int k = 0; k < 4; ++k) pv[k] = __builtin_bit_cast(f32x4, __builtin_amdgcn_raw_buffer_load_b128(prs, o + (unsigned)k * (S_ * D_ * 4u), 0, 16));
        const u32x2 xv = *(const u32x2*)(x + (size_t)tok * D_ + d);
        float r[4];
#pragma unroll
        for (int j = 0; j < 4; ++j) {
            const float sv = rbf(rbf(pv[0][j]) - rbf(pv[1][j]));
            const float dv = rbf(pv[2][j] + pv[3][j]);
            const float sl = rbf(dv * __builtin_amdgcn_rcpf(1.0f + __expf(-dv)));
            const float xf = (j & 1) ? hi_f(xv[j >> 1]) : lo_f(xv[j >> 1]);
            r[j] = xf + rbf(sv * sl);
        }
        u32x2 ov; ov[0] = cvt_pk(r[0], r[1]); ov[1] = cvt_pk(r[2], r[3]);
        *(u32x2*)(out + (size_t)tok * D_ + d) = ov;
    }
}

extern "C" void kernel_launch(void* const* d_in, const int* in_sizes, int n_in,
                              void* d_out, int out_size, void* d_ws, size_t ws_size,
                              hipStream_t stream) {
    const bf16_t* x = (const bf16_t*)d_in[0];
    const bf16_t* cre = (const bf16_t*)d_in[1];
    const bf16_t* cim = (const bf16_t*)d_in[2];
    const bf16_t* wre = (const bf16_t*)d_in[3];
    const bf16_t* wim = (const bf16_t*)d_in[4];
    const bf16_t* wg = (const bf16_t*)d_in[6];
    const bf16_t* wu = (const bf16_t*)d_in[7];
    const bf16_t* wd = (const bf16_t*)d_in[8];
    bf16_t* out = (bf16_t*)d_out;
    char* ws = (char*)d_ws;
    bf16_t* xn = (bf16_t*)ws;                       ws += (size_t)S_ * D_ * 2;
    bf16_t* abT = (bf16_t*)ws;                      ws += (size_t)2 * D_ * S_ * 2;
    bf16_t* hbuf = (bf16_t*)ws;                     ws += (size_t)S_ * F_ * 2;
    float* pbuf = (float*)ws;                       ws += (size_t)4 * S_ * D_ * 4;
    bf16_t* wdT = (bf16_t*)ws;                      ws += (size_t)D_ * F_ * 2;
    unsigned* cnt = (unsigned*)ws;                  ws += 64 * 32 * 4;
    ln_kernel<<<PREP_LN + PREP_TR + 2 * PREP_PFW + 2 * PREP_PFH, 256, 0, stream>>>(x, xn, wg, wu, wre, wim, wd, wdT, cnt);
    stage1_kernel<<<256, 512, 0, stream>>>(xn, wg, wu, wre, wim, hbuf, abT, cre, cim);
    stage2_kernel<<<256, (8 + S2_NL) * 64, 0, stream>>>(cre, cim, abT, hbuf, wdT, pbuf, cnt, x, out);
}
```
